```python
import torch

def _softmax(x, axis=-1):
    return torch.softmax(x, dim=axis)
def _gelu(x):
    return torch.nn.functional.gelu(x, approximate="tanh")
def _silu(x):
    return torch.nn.functional.silu(x)
def _sigmoid(x):
    return torch.sigmoid(x)
def _layernorm(x, axis=-1, eps=1e-5):
    m = x.mean(dim=axis, keepdim=True)
    v = x.var(dim=axis, keepdim=True, unbiased=False)
    return (x - m) / torch.sqrt(v + eps)
def _scatter_add(tgt, idx, src, axis=0):
    return tgt.clone().index_add_(axis, idx, src)
def _segment_sum(src, seg, n):
    out = torch.zeros((n,) + src.shape[1:], dtype=src.dtype)
    return out.index_add_(0, seg, src)
def _pad(x, pw):
    flat = tuple(v for pair in reversed(pw) for v in pair)
    return torch.nn.functional.pad(x, flat)

kernel_name = 'sequential_preLN__mha_block__moe_arch_mod__prompt'

def setup_inputs():

    _w = (lambda a: (lambda t: t.to(torch.bfloat16) if t.is_floating_point() else t)(torch.as_tensor(a)))
    def _A_setup():
        B, S, D, H = 1, 2048, 576, 32
        return {
            "x": _w((lambda *s: torch.randn(*s, dtype=torch.bfloat16) * 0.05)(B, S, D)),
            "wqkv": _w((lambda *s: torch.randn(*s, dtype=torch.bfloat16) * 0.05)(D, 3 * D)),
            "wo": _w((lambda *s: torch.randn(*s, dtype=torch.bfloat16) * 0.05)(D, D)),
        }
    _A_in = _A_setup()

    def _B_setup():
        B, S, D, F, C = 1, 2048, 576, 4608, 1
        return {
            "x": _w((lambda *s: torch.randn(*s, dtype=torch.bfloat16) * 0.05)(B, S, D)),
            "w_r": _w((lambda *s: torch.randn(*s, dtype=torch.bfloat16) * 0.05)(D)),
            "w_gate": _w((lambda *s: torch.randn(*s, dtype=torch.bfloat16) * 0.05)(D, F)),
            "w_up": _w((lambda *s: torch.randn(*s, dtype=torch.bfloat16) * 0.05)(D, F)),
            "w_down": _w((lambda *s: torch.randn(*s, dtype=torch.bfloat16) * 0.05)(F, D)),
        }
    _B_in = _B_setup()

    return {
        "A_x": _A_in["x"],
        "A_wqkv": _A_in["wqkv"],
        "A_wo": _A_in["wo"],
        "B_x": _B_in["x"],
        "B_w_r": _B_in["w_r"],
        "B_w_gate": _B_in["w_gate"],
        "B_w_up": _B_in["w_up"],
        "B_w_down": _B_in["w_down"],
    }

def reference(A_x, A_wqkv, A_wo, B_x, B_w_r, B_w_gate, B_w_up, B_w_down):
    def _A_ref(x, wqkv, wo):
        B, S, D = x.shape
        H = 32
        Dh = D // H
        qkv = (lambda x, *d: x.permute(*d))((x @ wqkv).reshape(B, S, 3, H, Dh), 2, 0, 3, 1, 4)
        q, k, v = qkv[0], qkv[1], qkv[2]
        scores = torch.einsum("bhqd,bhkd->bhqk", q, k) * (Dh ** -0.5)
        p = _softmax(scores, axis=-1)
        o = torch.einsum("bhqk,bhkd->bhqd", p, v)
        return (lambda x, *d: x.permute(*d))(o, 0, 2, 1, 3).reshape(B, S, D) @ wo

    def _B_ref(x, w_r, w_gate, w_up, w_down):
        B, S, D = x.shape
        C = 1
        r = x @ w_r
        idx = torch.argsort(-r, axis=-1)[:, :C]
        idx_e = (lambda x, n, axis: torch.repeat_interleave(x, n, dim=axis))(idx.reshape(B, C, 1), D, axis=2)
        x_sel = (lambda x, i, axis: torch.take_along_dim(x, i, dim=axis))(x, idx_e, axis=1)
        g = _silu(x_sel @ w_gate)
        u = x_sel @ w_up
        y = (g * u) @ w_down
        r_sel = _sigmoid((lambda x, i, axis: torch.take_along_dim(x, i, dim=axis))(r, idx, axis=1))
        y = y * r_sel.reshape(B, C, 1)
        b_off = (torch.arange(B) * S).reshape(B, 1)
        flat_idx = (idx + b_off).reshape(B * C)
        zero = (x * 0.0).reshape(B * S, D)
        upd = _scatter_add(zero, flat_idx, y.reshape(B * C, D), axis=0)
        return x + upd.reshape(B, S, D)

    x = A_x
    h = x + _A_ref(_layernorm(x, axis=-1), A_wqkv, A_wo)
    h2 = h + _B_ref(_layernorm(h, axis=-1), B_w_r, B_w_gate, B_w_up, B_w_down)
    return h2
```

```cpp
#include <hip/hip_runtime.h>
#include <hip/hip_bf16.h>

typedef __attribute__((ext_vector_type(8))) short bf16x8;
typedef __attribute__((ext_vector_type(4))) short s16x4;
typedef __attribute__((ext_vector_type(16))) float f32x16;
typedef __attribute__((ext_vector_type(4))) float f32x4;
typedef __attribute__((ext_vector_type(4))) unsigned u32x4;
typedef __attribute__((ext_vector_type(2))) unsigned u32x2;
typedef unsigned long long u64;
#define LAS __attribute__((address_space(3)))
#define GAS __attribute__((address_space(1)))
typedef LAS const char* lds_cptr;
typedef GAS u64 gu64;

constexpr int S_ = 2048, D_ = 576, H_ = 32, F_ = 4608;
constexpr int CHUNK_B = 19456;
constexpr int HEAD_B = 8 * CHUNK_B;
constexpr size_t WS_KV = 0;
constexpr size_t WS_Q1 = WS_KV + (size_t)H_ * HEAD_B;
constexpr size_t WS_Q2 = WS_Q1 + (size_t)H_ * 65536;
constexpr size_t WS_O = WS_Q2 + (size_t)H_ * 8192;
constexpr size_t WS_SYNC = WS_O + (size_t)S_ * D_ * 2;
constexpr int SY_STATS = 0;
constexpr int SY_BEST = SY_STATS + 256 * 192 * 8;
constexpr int SY_CHN = SY_BEST + 256;
constexpr int SY_CH = SY_CHN + 32 * 288 * 8;
constexpr int SY_PART = SY_CH + 32 * 288 * 8;
constexpr int SY_END = SY_PART + 144 * 576 * 8;
constexpr int SY_ZT = 295;
static_assert(256 * SY_ZT * 16 >= SY_END, "zero area");
constexpr unsigned TAG = 0x5A17u;

#define MFMA32(a, b, c) __builtin_amdgcn_mfma_f32_32x32x16_bf16(a, b, c, 0, 0, 0)
#define MFMA32K8(a, b, c) __builtin_amdgcn_mfma_f32_32x32x8bf16_1k(a, b, c, 0, 0, 0)
#define MFMA16(a, b, c) __builtin_amdgcn_mfma_f32_16x16x32_bf16(a, b, c, 0, 0, 0)
#define WAIT_BAR(N) asm volatile("s_waitcnt vmcnt(" #N ") lgkmcnt(0)\n\ts_barrier" ::: "memory")
#define LDS_BAR() asm volatile("s_waitcnt lgkmcnt(0)\n\ts_barrier" ::: "memory")
#define PIN(x) asm volatile("" : "+v"(x))

__device__ __forceinline__ unsigned cvtpk(float lo, float hi) {
  typedef __attribute__((ext_vector_type(2))) float f2_t;
  typedef __attribute__((ext_vector_type(2))) __bf16 b2_t;
  return __builtin_bit_cast(unsigned, __builtin_convertvector((f2_t{lo, hi}), b2_t));
}
__device__ __forceinline__ float bflo(unsigned u) { return __uint_as_float(u << 16); }
__device__ __forceinline__ float bfhi(unsigned u) { return __uint_as_float(u & 0xffff0000u); }
__device__ __forceinline__ float rbf(float x) { return bflo(cvtpk(x, x)); }
__device__ __forceinline__ void glds16(const void* sbase, unsigned voff, unsigned lds_dst) {
  unsigned sv;
  asm volatile("s_mov_b32 %0, m0\n\ts_mov_b32 m0, %3\n\ts_nop 0\n\tglobal_load_lds_dwordx4 %1, %2\n\ts_mov_b32 m0, %0"
               : "=&s"(sv) : "v"(voff), "s"(sbase), "s"(lds_dst) : "memory");
}
__device__ __forceinline__ u32x4 gload16(const void* sbase, unsigned voff) {
  u32x4 r; asm volatile("global_load_dwordx4 %0, %1, %2" : "=v"(r) : "v"(voff), "s"(sbase) : "memory"); return r;
}
__device__ __forceinline__ u32x2 gload8(const void* sbase, unsigned voff) {
  u32x2 r; asm volatile("global_load_dwordx2 %0, %1, %2" : "=v"(r) : "v"(voff), "s"(sbase) : "memory"); return r;
}
__device__ __forceinline__ unsigned gload4(const void* sbase, unsigned voff) {
  unsigned r; asm volatile("global_load_dword %0, %1, %2" : "=v"(r) : "v"(voff), "s"(sbase) : "memory"); return r;
}
__device__ __forceinline__ bf16x8 lds128(lds_cptr p) { return *(const LAS bf16x8*)p; }
__device__ __forceinline__ s16x4 ldstr(lds_cptr p) {
  return __builtin_amdgcn_ds_read_tr16_b64_v4i16((LAS s16x4*)p);
}
__device__ __forceinline__ bf16x8 cat8(s16x4 a, s16x4 b) { return __builtin_shufflevector(a, b, 0, 1, 2, 3, 4, 5, 6, 7); }
__device__ __forceinline__ void st_granule(char* p, unsigned v) {
  __hip_atomic_store((gu64*)p, ((u64)TAG << 32) | v, __ATOMIC_RELAXED, __HIP_MEMORY_SCOPE_AGENT);
}
__device__ __forceinline__ u64 ld_granule(const char* p) {
  return __hip_atomic_load((gu64*)p, __ATOMIC_RELAXED, __HIP_MEMORY_SCOPE_AGENT);
}
constexpr unsigned SPIN_CAP = 1u << 20;

constexpr int K1_APITCH = 1168;
constexpr int K1_WBUF = 27648;
constexpr int K1_WOFF = 64 * K1_APITCH;
constexpr int K1_LDS = K1_WOFF + 3 * K1_WBUF + 64;

template <bool TR>
__device__ __forceinline__ void k1_main(f32x16 (&acc)[2], lds_cptr ap, lds_cptr bp, const char* wq, const unsigned (&voff)[4],
                                        unsigned ldsw, int w) {
#pragma unroll
  for (int c = 0; c < 9; ++c) {
    if (c <= 6) { if (w < 3) { if (c == 0) WAIT_BAR(8); else WAIT_BAR(4); } else { if (c == 0) WAIT_BAR(6); else WAIT_BAR(3); } }
    else if (c == 7) { if (w < 3) WAIT_BAR(4); else WAIT_BAR(3); }
    else WAIT_BAR(0);
    if (c >= 1 && c + 2 < 9) {
      const char* src = wq + (size_t)(c + 2) * 64 * 3456;
      const unsigned dst = ldsw + ((c + 2) % 3) * K1_WBUF;
      glds16(src, voff[0], dst); glds16(src, voff[1], dst + 8192); glds16(src, voff[2], dst + 16384);
      if (w < 3) glds16(src, voff[3], dst + 24576);
    }
#pragma unroll
    for (int ks = 0; ks < 4; ++ks) {
      const lds_cptr b = bp + (c % 3) * K1_WBUF + ks * 6912;
      const bf16x8 wf = cat8(ldstr(b), ldstr(b + 432));
      const bf16x8 x0 = lds128(ap + c * 128 + ks * 32);
      const bf16x8 x1 = lds128(ap + 32 * K1_APITCH + c * 128 + ks * 32);
      if (TR) { acc[0] = MFMA32(wf, x0, acc[0]); acc[1] = MFMA32(wf, x1, acc[1]); }
      else    { acc[0] = MFMA32(x0, wf, acc[0]); acc[1] = MFMA32(x1, wf, acc[1]); }
    }
  }
}

__global__ void __launch_bounds__(512, 2) k1_qkv(const char* __restrict__ x, const char* __restrict__ wqkv, char* __restrict__ ws) {
  extern __shared__ __attribute__((aligned(16))) char smem[];
  const int tid = threadIdx.x, lane = tid & 63, w = __builtin_amdgcn_readfirstlane(tid >> 6);
  const int hg = blockIdx.x & 7, rb = blockIdx.x >> 3;
  const unsigned lds0 = (unsigned)(uintptr_t)smem;
  const int row = tid >> 3, j8 = tid & 7;
  u32x4 xr[9];
  {
    const char* xb = x + (size_t)rb * 64 * 1152;
    const unsigned vo = row * 1152 + j8 * 16;
#pragma unroll
    for (int i = 0; i < 9; ++i) xr[i] = gload16(xb, vo + i * 128);
  }
  unsigned voff[4];
#pragma unroll
  for (int p = 0; p < 4; ++p) {
    const int i = min((w + 8 * p) * 64 + lane, 1727);
    const int R = i / 27, cc = i - R * 27;
    const int k = (R & ~15) + 4 * (R & 3) + ((R >> 2) & 3);
    const int seg = cc / 9, wi = cc - seg * 9;
    voff[p] = k * 3456 + (seg * 576 + hg * 72 + wi * 8) * 2;
  }
  const unsigned ldsw = lds0 + K1_WOFF + w * 1024;
#pragma unroll
  for (int c = 0; c < 3; ++c) {
    const char* src = wqkv + (size_t)c * 64 * 3456;
    const unsigned dst = ldsw + c * K1_WBUF;
    glds16(src, voff[0], dst); glds16(src, voff[1], dst + 8192); glds16(src, voff[2], dst + 16384);
    if (w < 3) glds16(src, voff[3], dst + 24576);
  }
  if (w < 3) asm volatile("s_waitcnt vmcnt(12)" ::: "memory"); else asm volatile("s_waitcnt vmcnt(9)" ::: "memory");
#pragma unroll
  for (int i = 0; i < 9; ++i) PIN(xr[i]);
  {
    float s = 0.f;
#pragma unroll
    for (int i = 0; i < 9; ++i)
#pragma unroll
      for (int e = 0; e < 4; ++e) s += bflo(xr[i][e]) + bfhi(xr[i][e]);
    s += __shfl_xor(s, 1); s += __shfl_xor(s, 2); s += __shfl_xor(s, 4);
    const float m = s * (1.f / 576.f);
    float v = 0.f;
#pragma unroll
    for (int i = 0; i < 9; ++i)
#pragma unroll
      for (int e = 0; e < 4; ++e) { const float a = bflo(xr[i][e]) - m, b = bfhi(xr[i][e]) - m; v = fmaf(a, a, v); v = fmaf(b, b, v); }
    v += __shfl_xor(v, 1); v += __shfl_xor(v, 2); v += __shfl_xor(v, 4);
    const float mb = rbf(m), vb = rbf(v * (1.f / 576.f));
    const float den = rbf(sqrtf(rbf(vb + 1e-5f)));
    const float rinv = 1.f / den;
    LAS char* ap = (LAS char*)smem + row * K1_APITCH + j8 * 16;
#pragma unroll
    for (int i = 0; i < 9; ++i) {
      u32x4 o;
#pragma unroll
      for (int e = 0; e < 4; ++e) {
        const unsigned d = cvtpk(bflo(xr[i][e]) - mb, bfhi(xr[i][e]) - mb);
        o[e] = cvtpk(bflo(d) * rinv, bfhi(d) * rinv);
      }
      *(LAS u32x4*)(ap + i * 128) = o;
    }
  }
  const int h = lane >> 5, r32 = lane & 31;
  const int cb = (w < 5) ? 32 * w : 144 + 32 * (w - 5);
  const lds_cptr ap = (lds_cptr)smem + r32 * K1_APITCH + h * 16;
  const lds_cptr bp = (lds_cptr)smem + K1_WOFF + (4 * ((lane & 15) >> 2) + 2 * h) * 432 + (cb + 16 * ((lane >> 4) & 1) + 4 * (lane & 3)) * 2;
  f32x16 acc[2]; acc[0] = f32x16{}; acc[1] = f32x16{};
  char* kv = ws + WS_KV + (size_t)hg * 4 * HEAD_B;
  if (w < 5) {
    k1_main<true>(acc, ap, bp, wqkv, voff, ldsw, w);
    constexpr float QS = 0.23570226039551584f * 1.4426950408889634f;
#pragma unroll
    for (int rq = 0; rq < 4; ++rq) {
      const int G = 8 * w + 2 * rq + h;
      if (G < 36) {
        const int isk = G >= 18, Gs = G - 18 * isk, pr = Gs >= 9, r9 = Gs - 9 * pr;
        const float sc = isk ? 1.f : QS;
#pragma unroll
        for (int mi = 0; mi < 2; ++mi) {
          const int tile = 2 * rb + mi;
          const unsigned p0 = cvtpk(acc[mi][4 * rq] * sc, acc[mi][4 * rq + 1] * sc);
          const unsigned p1 = cvtpk(acc[mi][4 * rq + 2] * sc, acc[mi][4 * rq + 3] * sc);
          if (r9 != 4) {
            const int hd = 2 * pr + (r9 > 4), fg = r9 > 4 ? r9 - 5 : r9;
            const int in = ((fg >> 1) * 32 + r32) * 16 + (fg & 1) * 8;
            char* dst = isk ? kv + (size_t)hd * HEAD_B + (tile >> 3) * CHUNK_B + (tile & 7) * 1024 + in
                            : ws + WS_Q1 + (size_t)(hg * 4 + hd) * 65536 + tile * 1024 + in;
            *(u32x2*)dst = u32x2{p0, p1};
          } else {
            const int hd = 2 * pr;
            char* d0 = isk ? kv + (size_t)hd * HEAD_B + (tile >> 3) * CHUNK_B + 8192 + ((tile & 7) * 32 + r32) * 4
                           : ws + WS_Q2 + (size_t)(hg * 4 + hd) * 8192 + (tile * 32 + r32) * 4;
            char* d1 = d0 + (isk ? HEAD_B : 8192);
            *(unsigned*)d0 = p0; *(unsigned*)d1 = p1;
          }
        }
      }
    }
  } else {
    k1_main<false>(acc, ap, bp, wqkv, voff, ldsw, w);
    const int n = 32 * (w - 5) + r32;
    if (n < 76) {
      const int one = n >= 72;
      const int hd = one ? n - 72 : n / 18, d = one ? 18 : n - hd * 18;
#pragma unroll
      for (int mi = 0; mi < 2; ++mi) {
        const int tile = 2 * rb + mi;
        char* dst = kv + (size_t)hd * HEAD_B + (tile >> 3) * CHUNK_B + 9216 + (tile & 7) * 1280 + h * 320 + d * 16;
#pragma unroll
        for (int s = 0; s < 2; ++s) {
          u32x4 o;
#pragma unroll
          for (int e = 0; e < 4; ++e) o[e] = one ? 0x3F803F80u : cvtpk(acc[mi][8 * s + 2 * e], acc[mi][8 * s + 2 * e + 1]);
          *(u32x4*)(dst + s * 640) = o;
        }
      }
    }
  }
  if (tid < SY_ZT) *(u32x4*)(ws + WS_SYNC + ((size_t)blockIdx.x * SY_ZT + tid) * 16) = u32x4{0, 0, 0, 0};
}

constexpr int K2_LDS = HEAD_B + 64;

__device__ __forceinline__ void k2_load(const char* img, unsigned lds0, int w, int lane) {
#pragma unroll
  for (int i = 0; i < 10; ++i) {
    const int u = min(16 * i + w, 151);
    const int ph = u / 38, v = u - ph * 38;
    const int off = (v < 19) ? ph * CHUNK_B + v * 1024 : (4 + ph) * CHUNK_B + (v - 19) * 1024;
    glds16(img + off, lane * 16, lds0 + off);
  }
}

template <int MODE>
__device__ __forceinline__ void k2_tiles(lds_cptr kp, lds_cptr k2p, lds_cptr vp, bf16x8 qf1, s16x4 qf2, const f32x16& cin,
                                         f32x16& o, float& mx) {
#pragma unroll
  for (int t = 0; t < 8; ++t) {
    const bf16x8 kf1 = lds128(kp + t * 1024);
    const unsigned k2 = *(const LAS unsigned*)(k2p + t * 128);
    const s16x4 kf2 = __builtin_bit_cast(s16x4, u32x2{k2, 0u});
    f32x16 s = MFMA32(kf1, qf1, MODE == 1 ? cin : f32x16{});
    s = MFMA32K8(kf2, qf2, s);
    if (MODE == 2) {
#pragma unroll
      for (int r = 0; r < 16; ++r) mx = fmaxf(mx, s[r]);
    } else {
      const bf16x8 v0 = lds128(vp + t * 1280), v1 = lds128(vp + t * 1280 + 640);
      u32x4 p0, p1;
#pragma unroll
      for (int e = 0; e < 4; ++e) {
        p0[e] = cvtpk(__builtin_amdgcn_exp2f(s[2 * e]), __builtin_amdgcn_exp2f(s[2 * e + 1]));
        p1[e] = cvtpk(__builtin_amdgcn_exp2f(s[8 + 2 * e]), __builtin_amdgcn_exp2f(s[8 + 2 * e + 1]));
      }
      o = MFMA32(v0, __builtin_bit_cast(bf16x8, p0), o);
      o = MFMA32(v1, __builtin_bit_cast(bf16x8, p1), o);
    }
  }
}

__global__ void __launch_bounds__(1024, 4) k2_attn(char* __restrict__ ws) {
  extern __shared__ __attribute__((aligned(16))) char smem[];
  const int tid = threadIdx.x, lane = tid & 63, w = __builtin_amdgcn_readfirstlane(tid >> 6);
  const int head = (blockIdx.x & 7) * 4 + ((blockIdx.x >> 3) & 3), qp = blockIdx.x >> 5;
  const int qi = w & 7, kh = w >> 3, h = lane >> 5, r32 = lane & 31;
  const unsigned lds0 = (unsigned)(uintptr_t)smem;
  const char* img = ws + WS_KV + (size_t)head * HEAD_B;
  const int qt = qp * 8 + qi;
  u32x4 q1 = gload16(ws + WS_Q1 + (size_t)head * 65536 + qt * 1024, lane * 16);
  unsigned q2 = gload4(ws + WS_Q2 + (size_t)head * 8192 + qt * 128, r32 * 4);
  k2_load(img, lds0, w, lane);
  LAS unsigned* flag = (LAS unsigned*)(smem + HEAD_B);
  if (tid == 0) *flag = 0;
  const int drow = r32 < 19 ? r32 : ((r32 >= 20 && r32 < 28) ? 0 : 4);
  const lds_cptr kp = (lds_cptr)smem + kh * 4 * CHUNK_B + lane * 16;
  const lds_cptr k2p = (lds_cptr)smem + kh * 4 * CHUNK_B + 8192 + r32 * 4;
  const lds_cptr vp = (lds_cptr)smem + kh * 4 * CHUNK_B + 9216 + h * 320 + drow * 16;
  WAIT_BAR(7);
  PIN(q1); PIN(q2);
  const bf16x8 qf1 = __builtin_bit_cast(bf16x8, q1);
  const s16x4 qf2 = __builtin_bit_cast(s16x4, u32x2{h ? 0u : q2, 0u});
  f32x16 o = f32x16{};
  float mx = 0.f;
  const f32x16 zc = f32x16{};
#pragma unroll 1
  for (int ph = 0; ph < 4; ++ph) {
    if (ph == 1) WAIT_BAR(5); else if (ph == 2) WAIT_BAR(2); else if (ph == 3) WAIT_BAR(0);
    k2_tiles<0>(kp + ph * CHUNK_B, k2p + ph * CHUNK_B, vp + ph * CHUNK_B, qf1, qf2, zc, o, mx);
  }
  LAS float* cb = (LAS float*)smem + qi * 768 + lane;
  for (int attempt = 0;; ++attempt) {
    LDS_BAR();
    if (kh) {
#pragma unroll
      for (int r = 0; r < 12; ++r) cb[r * 64] = o[r];
    }
    LDS_BAR();
    float l = 1.f;
    if (!kh) {
#pragma unroll
      for (int r = 0; r < 12; ++r) o[r] += cb[r * 64];
      l = __shfl(o[10], r32);
      bool ok = (l > 8.6736174e-19f) && (l < 1.1529215e18f);
#pragma unroll
      for (int r = 0; r < 10; ++r) ok = ok && ((h && r >= 8) || fabsf(o[r]) < 3.0e38f);
      if (attempt == 0 && !__all(ok) && lane == 0) *flag = 1;
    }
    LDS_BAR();
    const unsigned redo = __builtin_amdgcn_readfirstlane(*flag);
    if (attempt == 1 || !redo) {
      if (!kh) {
        const float rl = 1.f / l;
        char* dst = ws + WS_O + (size_t)(qt * 32 + r32) * 1152 + head * 36 + h * 8;
        *(u32x2*)dst = u32x2{cvtpk(o[0] * rl, o[1] * rl), cvtpk(o[2] * rl, o[3] * rl)};
        *(u32x2*)(dst + 16) = u32x2{cvtpk(o[4] * rl, o[5] * rl), cvtpk(o[6] * rl, o[7] * rl)};
        if (!h) *(unsigned*)(dst + 32) = cvtpk(o[8] * rl, o[9] * rl);
      }
      break;
    }
    LDS_BAR();
    k2_load(img, lds0, w, lane);
    WAIT_BAR(0);
    mx = -INFINITY;
#pragma unroll 1
    for (int ph = 0; ph < 8; ++ph) {
      const int off = (ph - kh * 4) * CHUNK_B;
      k2_tiles<2>(kp + off, k2p + off, vp + off, qf1, qf2, zc, o, mx);
    }
    mx = fmaxf(mx, __shfl_xor(mx, 32));
    f32x16 cin;
#pragma unroll
    for (int r = 0; r < 16; ++r) cin[r] = -mx;
    o = f32x16{};
#pragma unroll 1
    for (int ph = 0; ph < 4; ++ph)
      k2_tiles<1>(kp + ph * CHUNK_B, k2p + ph * CHUNK_B, vp + ph * CHUNK_B, qf1, qf2, cin, o, mx);
  }
}

constexpr int K3_BOFF = 73728;
constexpr int K3_SOFF = K3_BOFF + 82944 + 64;
constexpr int K3_LDS = K3_SOFF + 6144 + 256;

__global__ void __launch_bounds__(512, 2) k3_tail(const char* __restrict__ x, const char* __restrict__ wo, const char* __restrict__ wr,
                                                  const char* __restrict__ wg, const char* __restrict__ wu, const char* __restrict__ wd,
                                                  char* __restrict__ out, char* __restrict__ ws) {
  extern __shared__ __attribute__((aligned(16))) char smem[];
  const int tid = threadIdx.x, lane = tid & 63, w = __builtin_amdgcn_readfirstlane(tid >> 6);
  const int b = blockIdx.x;
  const int panel = (b & 7) * 4 + ((b >> 3) & 3), cg = b >> 5;
  const unsigned lds0 = (unsigned)(uintptr_t)smem;
  char* sy = ws + WS_SYNC;
  const int mt = w & 3, nh = w >> 2, g = lane >> 4, l16 = lane & 15;
  const int nnt = nh ? 2 : 3, nt0 = nh ? 3 : 0;
  const int tok = panel * 64 + mt * 16 + l16;
  u32x2 xr[3], wrr[3];
#pragma unroll
  for (int i = 0; i < 3; ++i) {
    const int n = min((nt0 + i) * 16 + 4 * g, 68);
    xr[i] = gload8(x, tok * 1152 + (cg * 72 + n) * 2);
    wrr[i] = gload8(wr, (cg * 72 + n) * 2);
  }
  {
    const unsigned avo = (w * 8 + (lane >> 3)) * 1152 + (((lane & 7) ^ ((lane >> 4) & 3) ^ ((w & 1) << 2)) * 16);
    const char* asrc = ws + WS_O + (size_t)panel * 64 * 1152;
    unsigned bvo[4];
#pragma unroll
    for (int p = 0; p < 4; ++p) {
      const int i = min((w + 8 * p) * 64 + lane, 1727);
      const int R = i / 9, cc = i - R * 9;
      const int k = (R & ~12) | ((R & 4) << 1) | ((R & 8) >> 1);
      bvo[p] = k * 1152 + cg * 144 + cc * 16;
    }
#pragma unroll
    for (int th = 0; th < 3; ++th) {
#pragma unroll
      for (int kb = 0; kb < 3; ++kb) glds16(asrc + (th * 3 + kb) * 128, avo, lds0 + (th * 3 + kb) * 8192 + w * 1024);
      const char* bsrc = wo + (size_t)th * 192 * 1152;
      const unsigned bd = lds0 + K3_BOFF + th * 27648 + w * 1024;
      glds16(bsrc, bvo[0], bd); glds16(bsrc, bvo[1], bd + 8192); glds16(bsrc, bvo[2], bd + 16384);
      if (w < 3) glds16(bsrc, bvo[3], bd + 24576);
    }
  }
  const bool ffn = b < 144;
  const int fs = ffn ? (((b & 7) + 8 * (b >> 4)) * 2 + ((b >> 3) & 1)) : 0;
  u32x2 gw[9], uw[9];
  u32x4 dw[8];
  {
    const unsigned go = (tid >> 3) * (F_ * 2) + fs * 64 + (tid & 7) * 8;
#pragma unroll
    for (int i = 0; i < 9; ++i) { gw[i] = gload8(wg, go + i * 64 * (F_ * 2)); uw[i] = gload8(wu, go + i * 64 * (F_ * 2)); }
    const int t2 = min(tid, 287), rg = t2 / 72, c8 = t2 - rg * 72;
    const unsigned dn = (fs * 32 + rg * 8) * 1152 + c8 * 16;
#pragma unroll
    for (int i = 0; i < 8; ++i) dw[i] = gload16(wd, dn + i * 1152);
  }
  f32x4 acc[3];
#pragma unroll
  for (int i = 0; i < 3; ++i) acc[i] = f32x4{};
  {
    const int arow = mt * 16 + l16;
    const lds_cptr ap = (lds_cptr)smem + arow * 128;
    const int sw = (arow >> 1) & 7;
    const int a0 = ((g ^ sw) * 16), a1 = (((4 + g) ^ sw) * 16);
    const int q = l16 >> 2;
    const int r1 = (g >> 1) * 16 + (g & 1) * 4 + q;
    const lds_cptr bp = (lds_cptr)smem + K3_BOFF + r1 * 144 + nt0 * 32 + (lane & 3) * 8;
#pragma unroll
    for (int th = 0; th < 3; ++th) {
      if (w < 3) { if (th == 0) WAIT_BAR(40); else if (th == 1) WAIT_BAR(33); else WAIT_BAR(26); }
      else       { if (th == 0) WAIT_BAR(38); else if (th == 1) WAIT_BAR(32); else WAIT_BAR(26); }
#pragma unroll
      for (int ks = 0; ks < 6; ++ks) {
        const int kk = th * 6 + ks;
        const bf16x8 of = lds128(ap + (kk >> 1) * 8192 + ((kk & 1) ? a1 : a0));
#pragma unroll
        for (int i = 0; i < 3; ++i) {
          if (i < 2 || !nh) {
            const lds_cptr bb = bp + kk * 32 * 144 + i * 32;
            const bf16x8 wf = cat8(ldstr(bb), ldstr(bb + 8 * 144));
            acc[i] = MFMA16(wf, of, acc[i]);
          }
        }
      }
    }
  }
#pragma unroll
  for (int i = 0; i < 3; ++i) { PIN(xr[i]); PIN(wrr[i]); }
  float hv[3][4];
  LAS float* sc = (LAS float*)(smem + K3_SOFF);
  {
    float s1 = 0.f, s2 = 0.f, s3 = 0.f;
#pragma unroll
    for (int i = 0; i < 3; ++i) {
      const bool valid = (i < nnt) && ((nt0 + i) * 16 + 4 * g < 72);
      const unsigned a01 = cvtpk(acc[i][0], acc[i][1]), a23 = cvtpk(acc[i][2], acc[i][3]);
      const unsigned h01 = cvtpk(bflo(xr[i][0]) + bflo(a01), bfhi(xr[i][0]) + bfhi(a01));
      const unsigned h23 = cvtpk(bflo(xr[i][1]) + bflo(a23), bfhi(xr[i][1]) + bfhi(a23));
      hv[i][0] = valid ? bflo(h01) : 0.f; hv[i][1] = valid ? bfhi(h01) : 0.f; hv[i][2] = valid ? bflo(h23) : 0.f; hv[i][3] = valid ? bfhi(h23) : 0.f;
      const float w0 = bflo(wrr[i][0]), w1 = bfhi(wrr[i][0]), w2 = bflo(wrr[i][1]), w3 = bfhi(wrr[i][1]);
      s1 += (hv[i][0] + hv[i][1]) + (hv[i][2] + hv[i][3]);
      s2 = fmaf(hv[i][0], hv[i][0], fmaf(hv[i][1], hv[i][1], fmaf(hv[i][2], hv[i][2], fmaf(hv[i][3], hv[i][3], s2))));
      s3 = fmaf(hv[i][0], w0, fmaf(hv[i][1], w1, fmaf(hv[i][2], w2, fmaf(hv[i][3], w3, s3))));
    }
    s1 += __shfl_xor(s1, 16); s2 += __shfl_xor(s2, 16); s3 += __shfl_xor(s3, 16);
    s1 += __shfl_xor(s1, 32); s2 += __shfl_xor(s2, 32); s3 += __shfl_xor(s3, 32);
    if (g == 0) { sc[nh * 192 + mt * 16 + l16] = s1; sc[nh * 192 + 64 + mt * 16 + l16] = s2; sc[nh * 192 + 128 + mt * 16 + l16] = s3; }
  }
  LDS_BAR();
  if (tid < 192) st_granule(sy + SY_STATS + ((panel * 8 + cg) * 192 + tid) * 8, __float_as_uint(sc[tid] + sc[192 + tid]));
  LAS float* st = (LAS float*)smem;
  LAS float* wrs = (LAS float*)smem + 1536;
  for (int i = tid; i < 288; i += 512) { const unsigned u = *(const unsigned*)(wr + i * 4); wrs[2 * i] = bflo(u); wrs[2 * i + 1] = bfhi(u); }
  {
    const char* src = sy + SY_STATS + ((panel * 8 + w) * 192 + lane) * 8;
    u64 a, c, d;
    for (unsigned spin = 0; spin < SPIN_CAP; ++spin) {
      a = ld_granule(src); c = ld_granule(src + 512); d = ld_granule(src + 1024);
      if (__all((unsigned)(a >> 32) == TAG && (unsigned)(c >> 32) == TAG && (unsigned)(d >> 32) == TAG)) break;
      __builtin_amdgcn_s_sleep(1);
    }
    st[w * 192 + lane] = __uint_as_float((unsigned)a); st[w * 192 + 64 + lane] = __uint_as_float((unsigned)c); st[w * 192 + 128 + lane] = __uint_as_float((unsigned)d);
  }
  LDS_BAR();
  float mb_l, rinv_l; int bestrow; float bestr;
  {
    float s1 = 0.f, s2 = 0.f, s3 = 0.f, sw = 0.f;
#pragma unroll
    for (int i = 0; i < 8; ++i) { s1 += st[i * 192 + lane]; s2 += st[i * 192 + 64 + lane]; s3 += st[i * 192 + 128 + lane]; }
#pragma unroll
    for (int i = 0; i < 9; ++i) sw += wrs[i * 64 + lane];
#pragma unroll
    for (int o = 1; o < 64; o <<= 1) sw += __shfl_xor(sw, o);
    const float m = s1 * (1.f / 576.f);
    const float var = fmaxf(s2 * (1.f / 576.f) - m * m, 0.f);
    mb_l = rbf(m);
    rinv_l = 1.f / rbf(sqrtf(rbf(rbf(var) + 1e-5f)));
    float r = (s3 - mb_l * sw) * rinv_l;
    int idx = lane;
#pragma unroll
    for (int o = 1; o < 64; o <<= 1) {
      const float r2 = __shfl_xor(r, o); const int i2 = __shfl_xor(idx, o);
      if (r2 > r || (r2 == r && i2 < idx)) { r = r2; idx = i2; }
    }
    bestrow = __builtin_amdgcn_readfirstlane(idx); bestr = r;
  }
  if (cg == 0 && tid == 0) {
    st_granule(sy + SY_BEST + panel * 8, (__float_as_uint(bestr) & 0xFFFFFFC0u) | (unsigned)(63 - bestrow));
  }
  const float mb = __shfl(mb_l, mt * 16 + l16), rinv = __shfl(rinv_l, mt * 16 + l16);
  const bool isbest = (mt * 16 + l16) == bestrow;
  u32x2 ov[3];
#pragma unroll
  for (int i = 0; i < 3; ++i) {
    const bool valid = (i < nnt) && ((nt0 + i) * 16 + 4 * g < 72);
    const unsigned d01 = cvtpk(hv[i][0] - mb, hv[i][1] - mb), d23 = cvtpk(hv[i][2] - mb, hv[i][3] - mb);
    const unsigned n01 = cvtpk(bflo(d01) * rinv, bfhi(d01) * rinv), n23 = cvtpk(bflo(d23) * rinv, bfhi(d23) * rinv);
    ov[i] = u32x2{cvtpk(hv[i][0] + bflo(n01), hv[i][1] + bfhi(n01)), cvtpk(hv[i][2] + bflo(n23), hv[i][3] + bfhi(n23))};
    const int col = cg * 72 + (nt0 + i) * 16 + 4 * g;
    if (valid) {
      if (isbest) {
        st_granule(sy + SY_CHN + (panel * 288 + (col >> 1)) * 8, n01); st_granule(sy + SY_CHN + (panel * 288 + (col >> 1) + 1) * 8, n23);
        st_granule(sy + SY_CH + (panel * 288 + (col >> 1)) * 8, cvtpk(hv[i][0], hv[i][1])); st_granule(sy + SY_CH + (panel * 288 + (col >> 1) + 1) * 8, cvtpk(hv[i][2], hv[i][3]));
      } else {
        *(u32x2*)(out + (size_t)tok * 1152 + col * 2) = ov[i];
      }
    }
  }
  int wpanel;
  {
    u64 a = 0;
    const char* src = sy + SY_BEST + (lane & 31) * 8;
    for (unsigned spin = 0; spin < SPIN_CAP; ++spin) {
      a = ld_granule(src);
      if (__all((unsigned)(a >> 32) == TAG)) break;
      __builtin_amdgcn_s_sleep(1);
    }
    float r = __uint_as_float((unsigned)a & 0xFFFFFFC0u); int idx = lane & 31;
#pragma unroll
    for (int o = 1; o < 32; o <<= 1) {
      const float r2 = __shfl_xor(r, o); const int i2 = __shfl_xor(idx, o);
      if (r2 > r || (r2 == r && i2 < idx)) { r = r2; idx = i2; }
    }
    wpanel = __builtin_amdgcn_readfirstlane(idx);
    const int wrow = 63 - (int)(__shfl((unsigned)a, wpanel) & 63u);
    bestrow = wpanel * 64 + wrow;
  }
  bestrow = __builtin_amdgcn_readfirstlane(bestrow);
  if (wpanel != panel && isbest) {
#pragma unroll
    for (int i = 0; i < 3; ++i) {
      const bool valid = (i < nnt) && ((nt0 + i) * 16 + 4 * g < 72);
      if (valid) *(u32x2*)(out + (size_t)tok * 1152 + (cg * 72 + (nt0 + i) * 16 + 4 * g) * 2) = ov[i];
    }
  }
  if (b >= 240) return;
  LAS float* xs = (LAS float*)smem + 2304;
  if (tid < 320) {
    const int t2 = min(tid, 287);
    const char* src = sy + SY_CHN + (wpanel * 288 + t2) * 8;
    u64 a = 0;
    for (unsigned spin = 0; spin < SPIN_CAP; ++spin) {
      a = ld_granule(src);
      if (__all((unsigned)(a >> 32) == TAG)) break;
      __builtin_amdgcn_s_sleep(1);
    }
    xs[2 * t2] = bflo((unsigned)a); xs[2 * t2 + 1] = bfhi((unsigned)a);
  }
  if (ffn) {
    asm volatile("s_waitcnt vmcnt(0)" ::: "memory");
#pragma unroll
    for (int i = 0; i < 9; ++i) { PIN(gw[i]); PIN(uw[i]); }
#pragma unroll
    for (int i = 0; i < 8; ++i) PIN(dw[i]);
    LDS_BAR();
    float ga[4] = {0.f, 0.f, 0.f, 0.f}, ua[4] = {0.f, 0.f, 0.f, 0.f};
#pragma unroll
    for (int i = 0; i < 9; ++i) {
      const float xv = xs[(tid >> 3) + 64 * i];
      ga[0] = fmaf(xv, bflo(gw[i][0]), ga[0]); ga[1] = fmaf(xv, bfhi(gw[i][0]), ga[1]); ga[2] = fmaf(xv, bflo(gw[i][1]), ga[2]); ga[3] = fmaf(xv, bfhi(gw[i][1]), ga[3]);
      ua[0] = fmaf(xv, bflo(uw[i][0]), ua[0]); ua[1] = fmaf(xv, bfhi(uw[i][0]), ua[1]); ua[2] = fmaf(xv, bflo(uw[i][1]), ua[2]); ua[3] = fmaf(xv, bfhi(uw[i][1]), ua[3]);
    }
#pragma unroll
    for (int e = 0; e < 4; ++e) {
#pragma unroll
      for (int o = 8; o < 64; o <<= 1) { ga[e] += __shfl_xor(ga[e], o); ua[e] += __shfl_xor(ua[e], o); }
    }
    LAS float* gp = (LAS float*)smem + 2880;
    if (lane < 8) {
#pragma unroll
      for (int e = 0; e < 4; ++e) { gp[w * 64 + lane * 4 + e] = ga[e]; gp[w * 64 + 32 + lane * 4 + e] = ua[e]; }
    }
    LDS_BAR();
    LAS float* act = (LAS float*)smem + 3392;
    if (tid < 32) {
      float gs = 0.f, us = 0.f;
#pragma unroll
      for (int i = 0; i < 8; ++i) { gs += gp[i * 64 + tid]; us += gp[i * 64 + 32 + tid]; }
      gs = rbf(gs); us = rbf(us);
      const float sl = rbf(gs / (1.f + __expf(-gs)));
      act[tid] = rbf(sl * us);
    }
    LDS_BAR();
    LAS float* yp = (LAS float*)smem + 3456;
    if (tid < 288) {
      const int rg = tid / 72, c8 = tid - rg * 72;
      float y[8] = {0.f, 0.f, 0.f, 0.f, 0.f, 0.f, 0.f, 0.f};
#pragma unroll
      for (int i = 0; i < 8; ++i) {
        const float a = act[rg * 8 + i];
#pragma unroll
        for (int e = 0; e < 4; ++e) { y[2 * e] = fmaf(a, bflo(dw[i][e]), y[2 * e]); y[2 * e + 1] = fmaf(a, bfhi(dw[i][e]), y[2 * e + 1]); }
      }
#pragma unroll
      for (int e = 0; e < 8; ++e) yp[rg * 576 + c8 * 8 + e] = y[e];
    }
    LDS_BAR();
    for (int c = tid; c < 576; c += 512)
      st_granule(sy + SY_PART + (fs * 576 + c) * 8, __float_as_uint((yp[c] + yp[576 + c]) + (yp[1152 + c] + yp[1728 + c])));
  } else {
    const int c0 = (b - 144) * 6;
    LAS float* pp = (LAS float*)smem + 2880;
    u64 hh = 0;
    if (w == 7) {
      const char* src = sy + SY_CH + (wpanel * 288 + (c0 >> 1) + min(lane, 2)) * 8;
      for (unsigned spin = 0; spin < SPIN_CAP; ++spin) {
        hh = ld_granule(src);
        if (__all((unsigned)(hh >> 32) == TAG)) break;
        __builtin_amdgcn_s_sleep(1);
      }
    } else {
      const int t2 = min(tid, 431), p = t2 / 3, c2 = t2 - p * 3;
      const char* src = sy + SY_PART + (p * 576 + c0 + c2 * 2) * 8;
      u64 a = 0, c = 0;
      for (unsigned spin = 0; spin < SPIN_CAP; ++spin) {
        a = ld_granule(src); c = ld_granule(src + 8);
        if (__all((unsigned)(a >> 32) == TAG && (unsigned)(c >> 32) == TAG)) break;
        __builtin_amdgcn_s_sleep(1);
      }
      pp[(c2 * 2) * 144 + p] = __uint_as_float((unsigned)a); pp[(c2 * 2 + 1) * 144 + p] = __uint_as_float((unsigned)c);
    }
    LDS_BAR();
    if (w == 7) {
      float r = 0.f;
#pragma unroll
      for (int i = 0; i < 9; ++i) r = fmaf(xs[i * 64 + lane], wrs[i * 64 + lane], r);
#pragma unroll
      for (int o = 1; o < 64; o <<= 1) r += __shfl_xor(r, o);
      r = rbf(r);
      const float sig = rbf(1.f / (1.f + __expf(-r)));
      float res[6];
#pragma unroll
      for (int c = 0; c < 6; ++c) {
        float y = pp[c * 144 + lane] + pp[c * 144 + 64 + lane] + (lane < 16 ? pp[c * 144 + 128 + lane] : 0.f);
#pragma unroll
        for (int o = 1; o < 64; o <<= 1) y += __shfl_xor(y, o);
        const float upd = rbf(rbf(y) * sig);
        const unsigned hp = __shfl((unsigned)hh, c >> 1);
        res[c] = ((c & 1) ? bfhi(hp) : bflo(hp)) + rbf(xs[c0 + c] + upd);
      }
      if (lane == 0) {
        unsigned* dst = (unsigned*)(out + (size_t)bestrow * 1152 + c0 * 2);
        dst[0] = cvtpk(res[0], res[1]); dst[1] = cvtpk(res[2], res[3]); dst[2] = cvtpk(res[4], res[5]);
      }
    }
  }
}

extern "C" void kernel_launch(void* const* d_in, const int* in_sizes, int n_in, void* d_out, int out_size, void* d_ws,
                              size_t ws_size, hipStream_t stream) {
  const char* x = (const char*)d_in[0];
  const char* wqkv = (const char*)d_in[1];
  const char* wo = (const char*)d_in[2];
  const char* wr = (const char*)d_in[4];
  const char* wg = (const char*)d_in[5];
  const char* wu = (const char*)d_in[6];
  const char* wd = (const char*)d_in[7];
  char* ws = (char*)d_ws;
  (void)hipFuncSetAttribute((const void*)k1_qkv, hipFuncAttributeMaxDynamicSharedMemorySize, K1_LDS);
  (void)hipFuncSetAttribute((const void*)k2_attn, hipFuncAttributeMaxDynamicSharedMemorySize, K2_LDS);
  (void)hipFuncSetAttribute((const void*)k3_tail, hipFuncAttributeMaxDynamicSharedMemorySize, K3_LDS);
  k1_qkv<<<256, 512, K1_LDS, stream>>>(x, wqkv, ws);
  k2_attn<<<256, 1024, K2_LDS, stream>>>(ws);
  k3_tail<<<256, 512, K3_LDS, stream>>>(x, wo, wr, wg, wu, wd, (char*)d_out, ws);
}
```

```cpp
#include <hip/hip_runtime.h>
#include <hip/hip_bf16.h>

typedef __attribute__((ext_vector_type(8))) short bf16x8;
typedef __attribute__((ext_vector_type(4))) short s16x4;
typedef __attribute__((ext_vector_type(16))) float f32x16;
typedef __attribute__((ext_vector_type(4))) float f32x4;
typedef __attribute__((ext_vector_type(4))) unsigned u32x4;
typedef __attribute__((ext_vector_type(2))) unsigned u32x2;
typedef __attribute__((ext_vector_type(2))) __bf16 b2_t;
typedef unsigned long long u64;
#define LAS __attribute__((address_space(3)))
#define GAS __attribute__((address_space(1)))
typedef LAS const char* lds_cptr;
typedef LAS char* lds_ptr;
typedef GAS u64 gu64;
typedef GAS unsigned gu32;

constexpr int S_ = 2048, D_ = 576, H_ = 32, F_ = 4608;
constexpr int CHUNK_B = 19456;
constexpr int HEAD_B = 8 * CHUNK_B;
constexpr int WS_EPOCH = 0;
constexpr int WS_FLAG1 = 256;
constexpr int WS_FLAG2 = WS_FLAG1 + 2048;
constexpr int WS_KV = 8192;
constexpr int WS_Q1 = WS_KV + H_ * HEAD_B;
constexpr int WS_Q2 = WS_Q1 + H_ * 65536;
constexpr int WS_O1 = WS_Q2 + H_ * 8192;
constexpr int WS_O2 = WS_O1 + H_ * 65536;
constexpr int WS_SYNC = WS_O2 + S_ * 128;
constexpr int SY_STATS = WS_SYNC;
constexpr int SY_BEST = SY_STATS + 256 * 192 * 8;
constexpr int SY_CHN = SY_BEST + 256;
constexpr int SY_CH = SY_CHN + 32 * 288 * 8;
constexpr int SY_PART = SY_CH + 32 * 288 * 8;
constexpr int SY_END = SY_PART + 144 * 576 * 8;
constexpr int SY_ZT = 393;
static_assert(192 * SY_ZT * 16 >= SY_END - WS_SYNC, "clear area");
constexpr int WS_TOTAL = WS_SYNC + 192 * SY_ZT * 16;
constexpr unsigned TAG = 0x5A17u;

#define MFMA32(a, b, c) __builtin_amdgcn_mfma_f32_32x32x16_bf16(a, b, c, 0, 0, 0)
#define MFMA32K8(a, b, c) __builtin_amdgcn_mfma_f32_32x32x8bf16_1k(a, b, c, 0, 0, 0)
#define MFMA16(a, b, c) __builtin_amdgcn_mfma_f32_16x16x32_bf16(a, b, c, 0, 0, 0)
#define WAIT_VM(N) asm volatile("s_waitcnt vmcnt(" #N ")" ::: "memory")
#define WAIT_BAR(N) asm volatile("s_waitcnt vmcnt(" #N ") lgkmcnt(0)\n\ts_barrier" ::: "memory")
#define LDS_BAR() asm volatile("s_waitcnt lgkmcnt(0)\n\ts_barrier" ::: "memory")
#define PIN(x) asm volatile("" : "+v"(x))
#define SB() __builtin_amdgcn_sched_barrier(0)

__device__ __forceinline__ unsigned cvtpk(float lo, float hi) {
  typedef __attribute__((ext_vector_type(2))) float f2_t;
  return __builtin_bit_cast(unsigned, __builtin_convertvector((f2_t{lo, hi}), b2_t));
}
__device__ __forceinline__ float bflo(unsigned u) { return __uint_as_float(u << 16); }
__device__ __forceinline__ float bfhi(unsigned u) { return __uint_as_float(u & 0xffff0000u); }
__device__ __forceinline__ float rbf(float x) { return bflo(cvtpk(x, x)); }
__device__ __forceinline__ float dot2(unsigned a, unsigned b, float c) {
  return __builtin_amdgcn_fdot2_f32_bf16(__builtin_bit_cast(b2_t, a), __builtin_bit_cast(b2_t, b), c, false);
}
__device__ __forceinline__ void glds16(const void* sbase, unsigned voff, unsigned lds_dst) {
  unsigned sv;
  asm volatile("s_mov_b32 %0, m0\n\ts_mov_b32 m0, %3\n\ts_nop 0\n\tglobal_load_lds_dwordx4 %1, %2\n\ts_mov_b32 m0, %0"
               : "=&s"(sv) : "v"(voff), "s"(sbase), "s"(lds_dst) : "memory");
}
__device__ __forceinline__ u32x4 gload16(const void* sbase, unsigned voff) {
  u32x4 r; asm volatile("global_load_dwordx4 %0, %1, %2" : "=v"(r) : "v"(voff), "s"(sbase) : "memory"); return r;
}
__device__ __forceinline__ u32x2 gload8(const void* sbase, unsigned voff) {
  u32x2 r; asm volatile("global_load_dwordx2 %0, %1, %2" : "=v"(r) : "v"(voff), "s"(sbase) : "memory"); return r;
}
__device__ __forceinline__ u32x4 gload16s(const void* sbase, unsigned voff) {
  u32x4 r; asm volatile("global_load_dwordx4 %0, %1, %2 sc1" : "=v"(r) : "v"(voff), "s"(sbase) : "memory"); return r;
}
__device__ __forceinline__ u32x2 gload8s(const void* sbase, unsigned voff) {
  u32x2 r; asm volatile("global_load_dwordx2 %0, %1, %2 sc1" : "=v"(r) : "v"(voff), "s"(sbase) : "memory"); return r;
}
__device__ __forceinline__ unsigned gload4s(const void* sbase, unsigned voff) {
  unsigned r; asm volatile("global_load_dword %0, %1, %2 sc1" : "=v"(r) : "v"(voff), "s"(sbase) : "memory"); return r;
}
__device__ __forceinline__ void gstore16s(void* sbase, unsigned voff, u32x4 v) {
  asm volatile("global_store_dwordx4 %0, %1, %2 sc1\n\ts_nop 1" :: "v"(voff), "v"(v), "s"(sbase) : "memory");
}
__device__ __forceinline__ void gstore4s(void* sbase, unsigned voff, unsigned v) {
  asm volatile("global_store_dword %0, %1, %2 sc1" :: "v"(voff), "v"(v), "s"(sbase) : "memory");
}
__device__ __forceinline__ bf16x8 lds128(lds_cptr p) { return *(const LAS bf16x8*)p; }
__device__ __forceinline__ s16x4 ldstr(lds_cptr p) { return __builtin_amdgcn_ds_read_tr16_b64_v4i16((LAS s16x4*)p); }
__device__ __forceinline__ bf16x8 cat8(s16x4 a, s16x4 b) { return __builtin_shufflevector(a, b, 0, 1, 2, 3, 4, 5, 6, 7); }
__device__ __forceinline__ void st_u64(char* p, u64 v) { __hip_atomic_store((gu64*)p, v, __ATOMIC_RELAXED, __HIP_MEMORY_SCOPE_AGENT); }
__device__ __forceinline__ void st_granule(char* p, unsigned v) { st_u64(p, ((u64)TAG << 32) | v); }
__device__ __forceinline__ u64 ld_u64(const char* p) { return __hip_atomic_load((gu64*)p, __ATOMIC_RELAXED, __HIP_MEMORY_SCOPE_AGENT); }
constexpr unsigned SPIN_CAP = 1u << 18;
template <int CTRL> __device__ __forceinline__ unsigned dppu(unsigned x) { return (unsigned)__builtin_amdgcn_mov_dpp((int)x, CTRL, 0xf, 0xf, true); }
template <int CTRL> __device__ __forceinline__ float dppf(float x) { return __uint_as_float(dppu<CTRL>(__float_as_uint(x))); }
__device__ __forceinline__ float sum_x16(float x) { auto s = __builtin_amdgcn_permlane16_swap(__float_as_uint(x), __float_as_uint(x), false, false); return __uint_as_float(s[0]) + __uint_as_float(s[1]); }
__device__ __forceinline__ float sum_x32(float x) { auto s = __builtin_amdgcn_permlane32_swap(__float_as_uint(x), __float_as_uint(x), false, false); return __uint_as_float(s[0]) + __uint_as_float(s[1]); }
__device__ __forceinline__ float sum8(float x) { x += dppf<0xB1>(x); x += dppf<0x4E>(x); x += dppf<0x141>(x); return x; }
__device__ __forceinline__ float sum64(float x) { x = sum8(x); x += dppf<0x140>(x); return sum_x32(sum_x16(x)); }
__device__ __forceinline__ unsigned umax64(unsigned x) {
  x = max(x, dppu<0xB1>(x)); x = max(x, dppu<0x4E>(x)); x = max(x, dppu<0x141>(x)); x = max(x, dppu<0x140>(x));
  auto s = __builtin_amdgcn_permlane16_swap(x, x, false, false); x = max(s[0], s[1]);
  auto t = __builtin_amdgcn_permlane32_swap(x, x, false, false); return max(t[0], t[1]);
}
#define STAMP_DECL
#define STAMP(i)
#define STAMP_FLUSH(kid)

constexpr int P1_APITCH = 1168;
constexpr int P1_WBUF = 27648;
constexpr int P1_WOFF = 64 * P1_APITCH;
constexpr int LDS_BYTES = 163840;
static_assert(P1_WOFF + 3 * P1_WBUF + 128 <= LDS_BYTES, "P1 LDS");

#define WAIT2(lo, A, B) do { if (lo) WAIT_VM(A); else WAIT_VM(B); } while (0)

template <int MODE>
__device__ __forceinline__ void p1_main(f32x16 (&acc)[2], lds_cptr ap, lds_cptr bp, lds_ptr wdst, u32x4 (&wr)[6][4], int rot, bool lo) {
#pragma unroll
  for (int c = 0; c < 9; ++c) {
    if (c == 0) WAIT2(lo, 32, 24); else if (c == 1) WAIT2(lo, 28, 21); else if (c == 2) WAIT2(lo, 24, 18);
    LDS_BAR();
    if (MODE != 2) {
      int kc = c + rot; kc = kc >= 9 ? kc - 9 : kc;
      const lds_cptr a = ap + kc * 128;
#pragma unroll
      for (int ks = 0; ks < 4; ++ks) {
        const lds_cptr b = bp + (c % 3) * P1_WBUF + ks * 6912;
        const bf16x8 wf = cat8(ldstr(b), ldstr(b + 432));
        const bf16x8 x0 = lds128(a + ks * 32);
        const bf16x8 x1 = lds128(a + 32 * P1_APITCH + ks * 32);
        if (MODE == 0) { acc[0] = MFMA32(wf, x0, acc[0]); acc[1] = MFMA32(wf, x1, acc[1]); }
        else           { acc[0] = MFMA32(x0, wf, acc[0]); acc[1] = MFMA32(x1, wf, acc[1]); }
      }
    }
    if (c >= 2 && c < 8) {
      if (c == 2) WAIT2(lo, 20, 15); else if (c == 3) WAIT2(lo, 16, 12); else if (c == 4) WAIT2(lo, 12, 9);
      else if (c == 5) WAIT2(lo, 8, 6); else if (c == 6) WAIT2(lo, 4, 3); else WAIT_VM(0);
      const lds_ptr d = wdst + ((c + 1) % 3) * P1_WBUF;
#pragma unroll
      for (int p = 0; p < 3; ++p) { PIN(wr[c - 2][p]); *(LAS u32x4*)(d + p * 8192) = wr[c - 2][p]; }
      if (lo) { PIN(wr[c - 2][3]); *(LAS u32x4*)(d + 24576) = wr[c - 2][3]; }
    }
  }
}

struct AttnState { f32x16 sA, oA, oB; u32x4 pend; bf16x8 v1p, k1; s16x4 k2; };
#define EXP2(x) __builtin_amdgcn_exp2f(x)
#define G4(P, i, S, r) do { P[i] = cvtpk(EXP2(S[r]), EXP2(S[(r) + 1])); P[(i) + 1] = cvtpk(EXP2(S[(r) + 2]), EXP2(S[(r) + 3])); } while (0)
__device__ __forceinline__ s16x4 ldk2(lds_cptr p) { return __builtin_bit_cast(s16x4, u32x2{*(const LAS unsigned*)p, 0u}); }

__device__ __forceinline__ void attn8(AttnState& st, lds_cptr kp, lds_cptr k2p, lds_cptr vp, lds_cptr kn, lds_cptr k2n,
                                      bf16x8 qa1, s16x4 qa2, bf16x8 qb1, s16x4 qb2) {
#pragma unroll
  for (int t = 0; t < 8; ++t) {
    u32x4 p0, p1; f32x16 sB;
    const bf16x8 v0 = lds128(vp + t * 1280), v1 = lds128(vp + t * 1280 + 640);
    const bf16x8 k1n = lds128(t < 7 ? kp + (t + 1) * 1024 : kn);
    const s16x4 k2nx = ldk2(t < 7 ? k2p + (t + 1) * 128 : k2n);
    SB();
    st.oB = MFMA32(st.v1p, __builtin_bit_cast(bf16x8, st.pend), st.oB); SB();
    G4(p0, 0, st.sA, 0); SB();
    sB = MFMA32(st.k1, qb1, f32x16{}); SB();
    G4(p0, 2, st.sA, 4); SB();
    sB = MFMA32K8(st.k2, qb2, sB); SB();
    G4(p1, 0, st.sA, 8); SB();
    st.oA = MFMA32(v0, __builtin_bit_cast(bf16x8, p0), st.oA); SB();
    G4(p1, 2, st.sA, 12); SB();
    st.oA = MFMA32(v1, __builtin_bit_cast(bf16x8, p1), st.oA); SB();
    G4(p0, 0, sB, 0); SB();
    st.sA = MFMA32(k1n, qa1, f32x16{}); SB();
    G4(p0, 2, sB, 4); SB();
    st.sA = MFMA32K8(k2nx, qa2, st.sA); SB();
    G4(p1, 0, sB, 8); SB();
    st.oB = MFMA32(v0, __builtin_bit_cast(bf16x8, p0), st.oB); SB();
    G4(p1, 2, sB, 12); SB();
    st.pend = p1; st.v1p = v1; st.k1 = k1n; st.k2 = k2nx;
  }
  PIN(st.oA); PIN(st.oB); PIN(st.pend); PIN(st.sA);
}
template <int MODE>
__device__ __forceinline__ void attn_slow(lds_cptr kp, lds_cptr k2p, lds_cptr vp, bf16x8 qf1, s16x4 qf2, float negm, f32x16& o, float& mx) {
  f32x16 cin;
#pragma unroll
  for (int r = 0; r < 16; ++r) cin[r] = negm;
#pragma unroll 1
  for (int t = 0; t < 8; ++t) {
    f32x16 s = MFMA32(lds128(kp + t * 1024), qf1, cin);
    s = MFMA32K8(ldk2(k2p + t * 128), qf2, s);
    if (MODE == 2) {
#pragma unroll
      for (int r = 0; r < 16; ++r) mx = fmaxf(mx, s[r]);
    } else {
      u32x4 p0, p1;
      G4(p0, 0, s, 0); G4(p0, 2, s, 4); G4(p1, 0, s, 8); G4(p1, 2, s, 12);
      o = MFMA32(lds128(vp + t * 1280), __builtin_bit_cast(bf16x8, p0), o);
      o = MFMA32(lds128(vp + t * 1280 + 640), __builtin_bit_cast(bf16x8, p1), o);
    }
  }
}
__device__ __forceinline__ int kv_goff(int u) {
  const int slot = u / 19, pc = u - slot * 19;
  return ((slot & 1) * 4 + (slot >> 1)) * CHUNK_B + pc * 1024;
}
constexpr int P2_XOFF = 90112;
constexpr int P2_FLAG = HEAD_B;
constexpr int P3_XOFF = 84224;
constexpr int P3_SOFF = 106496;

__device__ __forceinline__ void wo_dma(const char* wo, int cg, unsigned lds0, int w, int lane) {
#pragma unroll
  for (int i = 0; i < 11; ++i) {
    if (i < 10 || w == 0) {
      const int ci = (8 * i + w) * 64 + lane;
      const int R = ci / 9, cc = ci - R * 9;
      glds16(wo, R * 1152 + cg * 144 + cc * 16, lds0 + (8 * i + w) * 1024);
    }
  }
}

__global__ void __launch_bounds__(512, 2) k_block(const char* __restrict__ x, const char* __restrict__ wqkv, const char* __restrict__ wo,
                                                  const char* __restrict__ wr, const char* __restrict__ wg, const char* __restrict__ wu,
                                                  const char* __restrict__ wd, char* __restrict__ out, char* __restrict__ ws) {
  extern __shared__ __attribute__((aligned(16))) char smem[];
  const int tid = threadIdx.x, lane = tid & 63, w = __builtin_amdgcn_readfirstlane(tid >> 6);
  const int b = blockIdx.x, xc = b & 7, sl = b >> 3;
  const unsigned lds0 = (unsigned)(uintptr_t)smem;
  const int h = lane >> 5, r32 = lane & 31;
  STAMP_DECL STAMP(0);
  u64 tag;
  {
    const int j = 4 * (xc >> 2) + (sl >> 3), rb = 8 * (xc & 3) + (sl & 7);
    u32x2 ep = gload8s(ws, WS_EPOCH);
    if (j >= 2 && tid < SY_ZT) gstore16s(ws, WS_SYNC + (((j - 2) * 32 + rb) * SY_ZT + tid) * 16, u32x4{0, 0, 0, 0});
    const int row = tid >> 3, j8 = tid & 7;
    u32x4 xr[9];
    {
      const char* xb = x + (size_t)rb * 64 * 1152;
      const unsigned vo = row * 1152 + j8 * 16;
#pragma unroll
      for (int i = 0; i < 9; ++i) xr[i] = gload16(xb, vo + i * 128);
    }
    unsigned voff[4];
#pragma unroll
    for (int p = 0; p < 4; ++p) {
      const int i = min((w + 8 * p) * 64 + lane, 1727);
      const int R = i / 27, cc = i - R * 27;
      const int k = (R & ~15) + 4 * (R & 3) + ((R >> 2) & 3);
      voff[p] = k * 3456 + j * 432 + cc * 16;
    }
    const bool lo = w < 3;
    const int rot = sl & 7;
    const unsigned ldsw = lds0 + P1_WOFF + w * 1024;
    u32x4 wrg[6][4];
#pragma unroll
    for (int c = 0; c < 9; ++c) {
      int kc = c + rot; kc = kc >= 9 ? kc - 9 : kc;
      const char* src = wqkv + (size_t)kc * (64 * 3456);
      if (c < 3) {
        const unsigned dst = ldsw + c * P1_WBUF;
        glds16(src, voff[0], dst); glds16(src, voff[1], dst + 8192); glds16(src, voff[2], dst + 16384);
        if (lo) glds16(src, voff[3], dst + 24576);
      } else {
        wrg[c - 3][0] = gload16(src, voff[0]); wrg[c - 3][1] = gload16(src, voff[1]); wrg[c - 3][2] = gload16(src, voff[2]);
        if (lo) wrg[c - 3][3] = gload16(src, voff[3]);
      }
    }
    WAIT2(lo, 36, 27);
#pragma unroll
    for (int i = 0; i < 9; ++i) PIN(xr[i]);
    PIN(ep);
    tag = (((u64)ep[1] << 32) | ep[0]) * 6364136223846793005ull + 1442695040888963407ull;
    STAMP(1);
    {
      float s = 0.f, q = 0.f;
#pragma unroll
      for (int i = 0; i < 9; ++i)
#pragma unroll
        for (int e = 0; e < 4; ++e) { s = dot2(xr[i][e], 0x3F803F80u, s); q = dot2(xr[i][e], xr[i][e], q); }
      s = sum8(s); q = sum8(q);
      const float m = s * (1.f / 576.f);
      const float var = fmaxf(fmaf(q, 1.f / 576.f, -m * m), 0.f);
      const float rinv = 1.f / rbf(sqrtf(rbf(rbf(var) + 1e-5f)));
      const float nb = -rbf(m) * rinv;
      const lds_ptr ap = (lds_ptr)smem + row * P1_APITCH + j8 * 16;
#pragma unroll
      for (int i = 0; i < 9; ++i) {
        u32x4 o;
#pragma unroll
        for (int e = 0; e < 4; ++e) o[e] = cvtpk(fmaf(bflo(xr[i][e]), rinv, nb), fmaf(bfhi(xr[i][e]), rinv, nb));
        *(LAS u32x4*)(ap + i * 128) = o;
      }
    }
    STAMP(2);
    const int npair = j < 5 ? 6 : (j == 5 ? 2 : 0);
    const int role = w < npair ? 0 : (npair && w == npair) ? 1 : (j < 5 || (j > 5 && w == 7)) ? 3 : 2;
    const int c0 = (j == 5) ? 72 + 32 * (w - 3) : 32 * w;
    const int sg = 4 * ((lane >> 4) & 1) + (lane & 3);
    const int col = role == 0 ? 36 * w + 4 * ((0x86753120u >> (4 * sg)) & 15) : role == 1 ? 36 * min(sg, npair - 1) + 16 : role == 2 ? c0 + 4 * sg : 0;
    const lds_cptr ap = (lds_cptr)smem + r32 * P1_APITCH + h * 16;
    const lds_cptr bp = (lds_cptr)smem + P1_WOFF + (4 * ((lane & 15) >> 2) + 2 * h) * 432 + col * 2;
    const lds_ptr wdst = (lds_ptr)smem + P1_WOFF + w * 1024 + lane * 16;
    f32x16 acc[2]; acc[0] = f32x16{}; acc[1] = f32x16{};
    constexpr float QS = 0.23570226039551584f * 1.4426950408889634f;
    const int t0 = 2 * rb;
    const int kvt = WS_KV + (t0 >> 3) * CHUNK_B;
    if (role <= 1) {
      p1_main<0>(acc, ap, bp, wdst, wrg, rot, lo);
      STAMP(3);
      if (role == 0) {
        const int P = 6 * j + w, isk = P >= 16, hd = 2 * (P & 15);
        const float sc = isk ? 1.f : QS;
        const int base = isk ? kvt + hd * HEAD_B + (t0 & 7) * 1024 : WS_Q1 + hd * 65536 + t0 * 1024;
        const int hs = isk ? HEAD_B : 65536;
#pragma unroll
        for (int mi = 0; mi < 2; ++mi)
#pragma unroll
          for (int ab = 0; ab < 2; ++ab) {
            u32x4 o;
#pragma unroll
            for (int e = 0; e < 4; ++e) o[e] = cvtpk(acc[mi][8 * ab + 2 * e] * sc, acc[mi][8 * ab + 2 * e + 1] * sc);
            gstore16s(ws, base + ab * hs + mi * 1024 + lane * 16, o);
          }
      } else {
#pragma unroll
        for (int rq = 0; rq < 3; ++rq) {
          const int t = 2 * rq + h;
          if (t < npair) {
            const int P = 6 * j + t, isk = P >= 16, hd = 2 * (P & 15);
            const float sc = isk ? 1.f : QS;
            const int base = (isk ? kvt + hd * HEAD_B + 8192 + (t0 & 7) * 128 : WS_Q2 + hd * 8192 + t0 * 128) + r32 * 4;
            const int hs = isk ? HEAD_B : 8192;
#pragma unroll
            for (int mi = 0; mi < 2; ++mi) {
              gstore4s(ws, base + mi * 128, cvtpk(acc[mi][4 * rq] * sc, acc[mi][4 * rq + 1] * sc));
              gstore4s(ws, base + hs + mi * 128, cvtpk(acc[mi][4 * rq + 2] * sc, acc[mi][4 * rq + 3] * sc));
            }
          }
        }
      }
    } else {
      if (role == 2) p1_main<1>(acc, ap, bp, wdst, wrg, rot, lo); else p1_main<2>(acc, ap, bp, wdst, wrg, rot, lo);
      if (j >= 5) {
        const int c = role == 2 ? c0 + r32 : 216 + r32;
        const bool one = c >= 216;
        const int vh0 = j == 5 ? 0 : 12 * j - 64, nh = j == 5 ? 8 : 12;
        const int vc = 216 * j + c - 1152;
        const int hd = one ? vh0 + (c - 216) : vc / 18, d = one ? 18 : vc - hd * 18;
        if (!one || (w == 7 && c - 216 < nh)) {
          const int base = kvt + hd * HEAD_B + 9216 + (t0 & 7) * 1280 + h * 320 + d * 16;
#pragma unroll
          for (int mi = 0; mi < 2; ++mi)
#pragma unroll
            for (int s = 0; s < 2; ++s) {
              u32x4 o;
#pragma unroll
              for (int e = 0; e < 4; ++e) o[e] = one ? 0x3F803F80u : cvtpk(acc[mi][8 * s + 2 * e], acc[mi][8 * s + 2 * e + 1]);
              gstore16s(ws, base + mi * 1280 + s * 640, o);
            }
        }
      }
    }
    WAIT_BAR(0);
    if (tid == 0) st_u64(ws + WS_FLAG1 + (j * 32 + rb) * 8, tag);
    STAMP(4);
  }
  const int panel = 4 * xc + (sl & 3), cg = sl >> 2;
  const int mt = w & 3, kq = w >> 2, g = lane >> 4, l16 = lane & 15;
  const int nnt = kq ? 2 : 3, nt0 = kq ? 3 : 0;
  const int tok = panel * 64 + mt * 16 + l16;
  const bool ffn = b < 144;
  const int fs = ((b & 7) + 8 * (b >> 4)) * 2 + ((b >> 3) & 1);
  u32x2 xres[3], wrr[3], gw[9], uw[9], wrow[2];
  u32x4 dw[8];
  {
    const int head = 4 * xc + (sl & 3), qp = sl >> 2;
    const int qpair = w & 3, kh = w >> 2;
    if (w == 0) {
      const char* f1 = ws + WS_FLAG1 + ((h ? (64 + head) / 12 : (32 + head) / 12) * 32 + r32) * 8;
      const char* f2 = ws + WS_FLAG1 + ((head / 12) * 32 + 4 * qp + (lane & 3)) * 8;
      for (unsigned spin = 0; spin < SPIN_CAP; ++spin) {
        const u64 a = ld_u64(f1), c = ld_u64(f2);
        if (__all(a == tag && c == tag)) break;
        __builtin_amdgcn_s_sleep(1);
      }
    }
    LDS_BAR();
    STAMP(5);
    const char* img = ws + WS_KV + (size_t)head * HEAD_B;
    const int qt = qp * 8 + 2 * qpair;
    u32x4 q1a = gload16s(ws, WS_Q1 + head * 65536 + qt * 1024 + lane * 16);
    u32x4 q1b = gload16s(ws, WS_Q1 + head * 65536 + qt * 1024 + 1024 + lane * 16);
    unsigned q2a = gload4s(ws, WS_Q2 + head * 8192 + qt * 128 + r32 * 4);
    unsigned q2b = gload4s(ws, WS_Q2 + head * 8192 + qt * 128 + 128 + r32 * 4);
    u32x4 kv[19];
#pragma unroll
    for (int i = 0; i < 19; ++i) kv[i] = gload16s(img + kv_goff(8 * i + w), lane * 16);
    const lds_ptr kdst = (lds_ptr)smem + w * 1024 + lane * 16;
    if (tid == 0) *(LAS unsigned*)(smem + P2_FLAG) = 0;
    WAIT_VM(14);
#pragma unroll
    for (int i = 0; i < 5; ++i) { PIN(kv[i]); *(LAS u32x4*)(kdst + i * 8192) = kv[i]; }
    PIN(q1a); PIN(q1b); PIN(q2a); PIN(q2b);
    const bf16x8 qa1 = __builtin_bit_cast(bf16x8, q1a), qb1 = __builtin_bit_cast(bf16x8, q1b);
    const s16x4 qa2 = __builtin_bit_cast(s16x4, u32x2{h ? 0u : q2a, 0u}), qb2 = __builtin_bit_cast(s16x4, u32x2{h ? 0u : q2b, 0u});
    const int drow = r32 < 19 ? r32 : ((r32 >= 20 && r32 < 28) ? 0 : 4);
    const lds_cptr kp = (lds_cptr)smem + kh * CHUNK_B + lane * 16;
    const lds_cptr k2p = (lds_cptr)smem + kh * CHUNK_B + 8192 + r32 * 4;
    const lds_cptr vp = (lds_cptr)smem + kh * CHUNK_B + 9216 + h * 320 + drow * 16;
    LDS_BAR();
    STAMP(6);
    AttnState st;
    st.oA = f32x16{}; st.oB = f32x16{}; st.pend = u32x4{0, 0, 0, 0}; st.v1p = bf16x8{};
    st.k1 = lds128(kp); st.k2 = ldk2(k2p);
    st.sA = MFMA32K8(st.k2, qa2, MFMA32(st.k1, qa1, f32x16{}));
    attn8(st, kp, k2p, vp, kp, k2p, qa1, qa2, qb1, qb2);
    WAIT_VM(0);
#pragma unroll
    for (int i = 5; i < 19; ++i) { PIN(kv[i]); *(LAS u32x4*)(kdst + i * 8192) = kv[i]; }
#pragma unroll
    for (int i = 0; i < 3; ++i) {
      const int n = min((nt0 + i) * 16 + 4 * g, 68);
      xres[i] = gload8(x, tok * 1152 + (cg * 72 + n) * 2);
      wrr[i] = gload8(wr, (cg * 72 + n) * 2);
    }
    wrow[0] = gload8(wr, lane * 8); wrow[1] = gload8(wr, 512 + lane * 8);
    unsigned wrt = gload4s(wr, 1024 + r32 * 4);
    if (ffn) {
      const unsigned go = (tid >> 3) * (F_ * 2) + fs * 64 + (tid & 7) * 8;
#pragma unroll
      for (int i = 0; i < 9; ++i) { gw[i] = gload8(wg, go + i * 64 * (F_ * 2)); uw[i] = gload8(wu, go + i * 64 * (F_ * 2)); }
      const int t2 = min(tid, 287), rg = t2 / 72, c8 = t2 - rg * 72;
      const unsigned dn = (fs * 32 + rg * 8) * 1152 + c8 * 16;
#pragma unroll
      for (int i = 0; i < 8; ++i) dw[i] = gload16(wd, dn + i * 1152);
    }
    LDS_BAR();
    st.k1 = lds128(kp + 2 * CHUNK_B); st.k2 = ldk2(k2p + 2 * CHUNK_B);
    st.sA = MFMA32K8(st.k2, qa2, MFMA32(st.k1, qa1, f32x16{}));
#pragma unroll 1
    for (int ph = 1; ph < 4; ++ph) {
      if (ph == 3) { LDS_BAR(); wo_dma(wo, cg, lds0, w, lane); }
      const int o = ph * 2 * CHUNK_B, on = ph < 3 ? o + 2 * CHUNK_B : o;
      attn8(st, kp + o, k2p + o, vp + o, kp + on, k2p + on, qa1, qa2, qb1, qb2);
    }
    st.oB = MFMA32(st.v1p, __builtin_bit_cast(bf16x8, st.pend), st.oB);
    STAMP(7);
    LAS float* xw = (LAS float*)(smem + P2_XOFF) + (qpair * 2 + kh) * 768 + lane;
    LAS float* xr_ = (LAS float*)(smem + P2_XOFF) + (qpair * 2 + (kh ^ 1)) * 768 + lane;
    for (int attempt = 0;; ++attempt) {
      f32x16 o = kh ? st.oB : st.oA;
      {
        const f32x16 give = kh ? st.oA : st.oB;
#pragma unroll
        for (int r = 0; r < 12; ++r) xw[r * 64] = give[r];
      }
      LDS_BAR();
#pragma unroll
      for (int r = 0; r < 12; ++r) o[r] += xr_[r * 64];
      auto sw = __builtin_amdgcn_permlane32_swap(__float_as_uint(o[10]), __float_as_uint(o[10]), false, false);
      const float l = __uint_as_float(sw[0]);
      bool ok = (l > 8.6736174e-19f) && (l < 1.1529215e18f);
#pragma unroll
      for (int r = 0; r < 10; ++r) ok = ok && ((h && r >= 8) || fabsf(o[r]) < 3.0e38f);
      if (attempt == 0 && !__all(ok) && lane == 0) *(LAS unsigned*)(smem + P2_FLAG) = 1;
      const float rl = 1.f / l;
      u32x4 ov;
#pragma unroll
      for (int e = 0; e < 4; ++e) ov[e] = cvtpk(o[2 * e] * rl, o[2 * e + 1] * rl);
      gstore16s(ws, WS_O1 + head * 65536 + (qt + kh) * 1024 + lane * 16, ov);
      if (!h) gstore4s(ws, WS_O2 + ((qt + kh) * 32 + r32) * 128 + head * 4, cvtpk(o[8] * rl, o[9] * rl));
      WAIT_BAR(0);
      const unsigned redo = __builtin_amdgcn_readfirstlane(*(LAS unsigned*)(smem + P2_FLAG));
      if (attempt == 1 || !redo) break;
      LDS_BAR();
#pragma unroll 1
      for (int i = 0; i < 19; ++i) {
        u32x4 t = gload16s(img + kv_goff(8 * i + w), lane * 16);
        WAIT_VM(0); PIN(t);
        *(LAS u32x4*)(kdst + i * 8192) = t;
      }
      LDS_BAR();
      float ma = -INFINITY, mb2 = -INFINITY;
#pragma unroll 1
      for (int sl2 = 0; sl2 < 8; ++sl2) {
        const int o2 = (sl2 - kh) * CHUNK_B;
        attn_slow<2>(kp + o2, k2p + o2, vp + o2, qa1, qa2, 0.f, st.oA, ma);
        attn_slow<2>(kp + o2, k2p + o2, vp + o2, qb1, qb2, 0.f, st.oA, mb2);
      }
      ma = fmaxf(ma, __shfl_xor(ma, 32)); mb2 = fmaxf(mb2, __shfl_xor(mb2, 32));
      st.oA = f32x16{}; st.oB = f32x16{};
#pragma unroll 1
      for (int ph = 0; ph < 4; ++ph) {
        const int o2 = ph * 2 * CHUNK_B;
        attn_slow<1>(kp + o2, k2p + o2, vp + o2, qa1, qa2, -ma, st.oA, ma);
        attn_slow<1>(kp + o2, k2p + o2, vp + o2, qb1, qb2, -mb2, st.oB, mb2);
      }
      LDS_BAR();
      wo_dma(wo, cg, lds0, w, lane);
    }
    if (tid == 0) st_u64(ws + WS_FLAG2 + (qp * 32 + head) * 8, tag);
    PIN(wrt); PIN(wrow[0]); PIN(wrow[1]);
    LAS float* wrs = (LAS float*)(smem + P3_SOFF) + 1920;
    if (w == 1) {
      *(LAS f32x4*)(wrs + lane * 4) = f32x4{bflo(wrow[0][0]), bfhi(wrow[0][0]), bflo(wrow[0][1]), bfhi(wrow[0][1])};
      *(LAS f32x4*)(wrs + 256 + lane * 4) = f32x4{bflo(wrow[1][0]), bfhi(wrow[1][0]), bflo(wrow[1][1]), bfhi(wrow[1][1])};
      if (!h) { wrs[512 + 2 * r32] = bflo(wrt); wrs[513 + 2 * r32] = bfhi(wrt); }
    }
    wrow[0][0] = __float_as_uint(sum64(dot2(wrow[0][0], 0x3F803F80u, dot2(wrow[0][1], 0x3F803F80u, dot2(wrow[1][0], 0x3F803F80u,
                                       dot2(wrow[1][1], 0x3F803F80u, dot2(h ? 0u : wrt, 0x3F803F80u, 0.f)))))));
    STAMP(8);
  }
  const float wsum = __uint_as_float(wrow[0][0]);
  LAS float* sc = (LAS float*)(smem + P3_SOFF);
  LAS float* wrs = sc + 1920;
  if (w == 0) {
    const char* f = ws + WS_FLAG2 + (xc * 32 + r32) * 8;
    for (unsigned spin = 0; spin < SPIN_CAP; ++spin) {
      if (__all(ld_u64(f) == tag)) break;
      __builtin_amdgcn_s_sleep(1);
    }
  }
  LDS_BAR();
  STAMP(9);
  f32x4 acc[5];
  {
    u32x4 of[9];
    const unsigned o1 = WS_O1 + (g >> 1) * 65536 + (2 * panel + (mt >> 1)) * 1024 + (g & 1) * 512 + ((mt & 1) * 16 + l16) * 16;
    const unsigned o2 = WS_O2 + tok * 128 + g * 16;
    if (kq == 0) {
#pragma unroll
      for (int i = 0; i < 9; ++i) of[i] = gload16s(ws, o1 + i * 131072);
    } else {
#pragma unroll
      for (int i = 0; i < 7; ++i) of[i] = gload16s(ws, o1 + (9 + i) * 131072);
      of[7] = gload16s(ws, o2); of[8] = gload16s(ws, o2 + 64);
    }
    const int q = l16 >> 2;
    const lds_cptr bp = (lds_cptr)smem + (kq * 9 * 36 + 18 * (g >> 1) + 4 * (g & 1) + q) * 144 + (lane & 3) * 8;
    const lds_cptr bp2 = (lds_cptr)smem + (18 * (4 * g + (q >> 1)) + 16 + (q & 1)) * 144 + (lane & 3) * 8;
#pragma unroll
    for (int i = 0; i < 5; ++i) acc[i] = f32x4{};
#pragma unroll
    for (int ks = 0; ks < 9; ++ks) {
      if (ks == 0) WAIT_VM(6); else if (ks == 3) WAIT_VM(3); else if (ks == 6) WAIT_VM(0);
      PIN(of[ks]);
      const bool sp = ks >= 7 && kq;
      const lds_cptr b0 = ks >= 7 ? (sp ? bp2 + (ks - 7) * (288 * 144) : bp + ks * (36 * 144)) : bp + ks * (36 * 144);
      const int d2 = sp ? 36 * 144 : 8 * 144;
#pragma unroll
      for (int i = 0; i < 5; ++i) {
        const bf16x8 wf = cat8(ldstr(b0 + i * 32), ldstr(b0 + d2 + i * 32));
        acc[i] = MFMA16(wf, __builtin_bit_cast(bf16x8, of[ks]), acc[i]);
      }
    }
    LAS f32x4* xw = (LAS f32x4*)(smem + P3_XOFF) + mt * 320 + lane;
    if (kq == 0) { xw[3 * 64] = acc[3]; xw[4 * 64] = acc[4]; } else { xw[0] = acc[0]; xw[64] = acc[1]; xw[128] = acc[2]; }
    LDS_BAR();
    if (kq == 0) { acc[0] += xw[0]; acc[1] += xw[64]; acc[2] += xw[128]; }
    else { acc[0] = acc[3] + xw[3 * 64]; acc[1] = acc[4] + xw[4 * 64]; acc[2] = f32x4{}; }
  }
#pragma unroll
  for (int i = 0; i < 3; ++i) { PIN(xres[i]); PIN(wrr[i]); }
  STAMP(10);
  float hv[3][4];
  {
    float s1 = 0.f, s2 = 0.f, s3 = 0.f;
#pragma unroll
    for (int i = 0; i < 3; ++i) {
      const bool valid = (i < nnt) && ((nt0 + i) * 16 + 4 * g < 72);
      const unsigned a01 = cvtpk(acc[i][0], acc[i][1]), a23 = cvtpk(acc[i][2], acc[i][3]);
      unsigned h01 = cvtpk(bflo(xres[i][0]) + bflo(a01), bfhi(xres[i][0]) + bfhi(a01));
      unsigned h23 = cvtpk(bflo(xres[i][1]) + bflo(a23), bfhi(xres[i][1]) + bfhi(a23));
      h01 = valid ? h01 : 0u; h23 = valid ? h23 : 0u;
      hv[i][0] = bflo(h01); hv[i][1] = bfhi(h01); hv[i][2] = bflo(h23); hv[i][3] = bfhi(h23);
      s1 += (hv[i][0] + hv[i][1]) + (hv[i][2] + hv[i][3]);
      s2 = fmaf(hv[i][0], hv[i][0], fmaf(hv[i][1], hv[i][1], fmaf(hv[i][2], hv[i][2], fmaf(hv[i][3], hv[i][3], s2))));
      s3 = fmaf(hv[i][0], bflo(wrr[i][0]), fmaf(hv[i][1], bfhi(wrr[i][0]), fmaf(hv[i][2], bflo(wrr[i][1]), fmaf(hv[i][3], bfhi(wrr[i][1]), s3))));
    }
    s1 = sum_x32(sum_x16(s1)); s2 = sum_x32(sum_x16(s2)); s3 = sum_x32(sum_x16(s3));
    if (g < 3) sc[kq * 192 + g * 64 + mt * 16 + l16] = g == 0 ? s1 : g == 1 ? s2 : s3;
  }
  LDS_BAR();
  if (tid < 192) st_granule(ws + SY_STATS + ((panel * 8 + cg) * 192 + tid) * 8, __float_as_uint(sc[tid] + sc[192 + tid]));
  LAS float* st = sc + 384;
  {
    const char* src = ws + SY_STATS + ((panel * 8 + w) * 192 + lane) * 8;
    u64 a, c, d;
    for (unsigned spin = 0; spin < SPIN_CAP; ++spin) {
      a = ld_u64(src); c = ld_u64(src + 512); d = ld_u64(src + 1024);
      if (__all((unsigned)(a >> 32) == TAG && (unsigned)(c >> 32) == TAG && (unsigned)(d >> 32) == TAG)) break;
      __builtin_amdgcn_s_sleep(1);
    }
    st[w * 192 + lane] = __uint_as_float((unsigned)a); st[w * 192 + 64 + lane] = __uint_as_float((unsigned)c); st[w * 192 + 128 + lane] = __uint_as_float((unsigned)d);
  }
  LDS_BAR();
  STAMP(11);
  unsigned key; float mb, rinv;
  {
    float s1 = 0.f, s2 = 0.f, s3 = 0.f, t1 = 0.f, t2 = 0.f;
    const int myr = mt * 16 + l16;
#pragma unroll
    for (int i = 0; i < 8; ++i) {
      s1 += st[i * 192 + lane]; s2 += st[i * 192 + 64 + lane]; s3 += st[i * 192 + 128 + lane];
      t1 += st[i * 192 + myr]; t2 += st[i * 192 + 64 + myr];
    }
    const float m = s1 * (1.f / 576.f);
    const float ri = 1.f / rbf(sqrtf(rbf(rbf(fmaxf(fmaf(s2, 1.f / 576.f, -m * m), 0.f)) + 1e-5f)));
    const unsigned u = __float_as_uint((s3 - rbf(m) * wsum) * ri);
    const unsigned ord = (u & 0x80000000u) ? ~u : (u | 0x80000000u);
    key = umax64((ord & ~0x7FFu) | (unsigned)(2047 - (panel * 64 + lane)));
    const float m2 = t1 * (1.f / 576.f);
    mb = rbf(m2);
    rinv = 1.f / rbf(sqrtf(rbf(rbf(fmaxf(fmaf(t2, 1.f / 576.f, -m2 * m2), 0.f)) + 1e-5f)));
  }
  if (cg == 0 && tid == 0) st_granule(ws + SY_BEST + panel * 8, key);
  const bool isbest = tok == 2047 - (int)(key & 0x7FFu);
  u32x2 ov[3];
#pragma unroll
  for (int i = 0; i < 3; ++i) {
    const bool valid = (i < nnt) && ((nt0 + i) * 16 + 4 * g < 72);
    const unsigned d01 = cvtpk(hv[i][0] - mb, hv[i][1] - mb), d23 = cvtpk(hv[i][2] - mb, hv[i][3] - mb);
    const unsigned n01 = cvtpk(bflo(d01) * rinv, bfhi(d01) * rinv), n23 = cvtpk(bflo(d23) * rinv, bfhi(d23) * rinv);
    ov[i] = u32x2{cvtpk(hv[i][0] + bflo(n01), hv[i][1] + bfhi(n01)), cvtpk(hv[i][2] + bflo(n23), hv[i][3] + bfhi(n23))};
    const int col = cg * 72 + (nt0 + i) * 16 + 4 * g;
    if (valid && isbest) {
      st_granule(ws + SY_CHN + (panel * 288 + (col >> 1)) * 8, n01); st_granule(ws + SY_CHN + (panel * 288 + (col >> 1) + 1) * 8, n23);
      st_granule(ws + SY_CH + (panel * 288 + (col >> 1)) * 8, cvtpk(hv[i][0], hv[i][1])); st_granule(ws + SY_CH + (panel * 288 + (col >> 1) + 1) * 8, cvtpk(hv[i][2], hv[i][3]));
    }
  }
#pragma unroll
  for (int i = 0; i < 3; ++i) {
    const bool valid = (i < nnt) && ((nt0 + i) * 16 + 4 * g < 72);
    if (valid && !isbest) *(u32x2*)(out + (size_t)tok * 1152 + (cg * 72 + (nt0 + i) * 16 + 4 * g) * 2) = ov[i];
  }
  int sel;
  {
    u64 a = 0;
    const char* src = ws + SY_BEST + r32 * 8;
    for (unsigned spin = 0; spin < SPIN_CAP; ++spin) {
      a = ld_u64(src);
      if (__all((unsigned)(a >> 32) == TAG)) break;
      __builtin_amdgcn_s_sleep(1);
    }
    sel = __builtin_amdgcn_readfirstlane(2047 - (int)(umax64((unsigned)a) & 0x7FFu));
  }
  const int wpanel = sel >> 6;
  if (wpanel != panel && isbest) {
#pragma unroll
    for (int i = 0; i < 3; ++i) {
      const bool valid = (i < nnt) && ((nt0 + i) * 16 + 4 * g < 72);
      if (valid) *(u32x2*)(out + (size_t)tok * 1152 + (cg * 72 + (nt0 + i) * 16 + 4 * g) * 2) = ov[i];
    }
  }
  STAMP(12);
  if (b >= 240) { STAMP_FLUSH(0); return; }
  LAS float* xs = sc + 2496;
  if (tid < 320) {
    const int t2 = min(tid, 287);
    const char* src = ws + SY_CHN + (wpanel * 288 + t2) * 8;
    u64 a = 0;
    for (unsigned spin = 0; spin < SPIN_CAP; ++spin) {
      a = ld_u64(src);
      if (__all((unsigned)(a >> 32) == TAG)) break;
      __builtin_amdgcn_s_sleep(1);
    }
    xs[2 * t2] = bflo((unsigned)a); xs[2 * t2 + 1] = bfhi((unsigned)a);
  }
  if (ffn) {
#pragma unroll
    for (int i = 0; i < 9; ++i) { PIN(gw[i]); PIN(uw[i]); }
#pragma unroll
    for (int i = 0; i < 8; ++i) PIN(dw[i]);
    LDS_BAR();
    STAMP(13);
    float ga[4] = {0.f, 0.f, 0.f, 0.f}, ua[4] = {0.f, 0.f, 0.f, 0.f};
#pragma unroll
    for (int i = 0; i < 9; ++i) {
      const float xv = xs[(tid >> 3) + 64 * i];
      ga[0] = fmaf(xv, bflo(gw[i][0]), ga[0]); ga[1] = fmaf(xv, bfhi(gw[i][0]), ga[1]); ga[2] = fmaf(xv, bflo(gw[i][1]), ga[2]); ga[3] = fmaf(xv, bfhi(gw[i][1]), ga[3]);
      ua[0] = fmaf(xv, bflo(uw[i][0]), ua[0]); ua[1] = fmaf(xv, bfhi(uw[i][0]), ua[1]); ua[2] = fmaf(xv, bflo(uw[i][1]), ua[2]); ua[3] = fmaf(xv, bfhi(uw[i][1]), ua[3]);
    }
    LAS float* gp = sc + 3072;
#pragma unroll
    for (int e = 0; e < 4; ++e) { ga[e] = sum_x32(sum_x16(ga[e])); ua[e] = sum_x32(sum_x16(ua[e])); }
    if (lane < 16) {
      *(LAS f32x4*)(gp + (w * 2 + (lane >> 3)) * 64 + (lane & 7) * 4) = f32x4{ga[0], ga[1], ga[2], ga[3]};
      *(LAS f32x4*)(gp + (w * 2 + (lane >> 3)) * 64 + 32 + (lane & 7) * 4) = f32x4{ua[0], ua[1], ua[2], ua[3]};
    }
    LDS_BAR();
    float av;
    {
      float s = 0.f;
#pragma unroll
      for (int i = 0; i < 16; ++i) s += gp[i * 64 + lane];
      s = rbf(s);
      auto sw = __builtin_amdgcn_permlane32_swap(__float_as_uint(s), __float_as_uint(s), false, false);
      const float gs = __uint_as_float(sw[0]), us = __uint_as_float(sw[1]);
      av = rbf(rbf(gs / (1.f + __expf(-gs))) * us);
    }
    LAS float* yp = sc + 4096;
    if (tid < 288) {
      const int rg = tid / 72, c8 = tid - rg * 72;
      float y[8] = {0.f, 0.f, 0.f, 0.f, 0.f, 0.f, 0.f, 0.f};
#pragma unroll
      for (int i = 0; i < 8; ++i) {
        const float a = __shfl(av, rg * 8 + i);
#pragma unroll
        for (int e = 0; e < 4; ++e) { y[2 * e] = fmaf(a, bflo(dw[i][e]), y[2 * e]); y[2 * e + 1] = fmaf(a, bfhi(dw[i][e]), y[2 * e + 1]); }
      }
      *(LAS f32x4*)(yp + rg * 576 + c8 * 8) = f32x4{y[0], y[1], y[2], y[3]};
      *(LAS f32x4*)(yp + rg * 576 + c8 * 8 + 4) = f32x4{y[4], y[5], y[6], y[7]};
    }
    LDS_BAR();
    if (tid < 288) {
      const int c = 2 * tid;
      const float y0 = (yp[c] + yp[576 + c]) + (yp[1152 + c] + yp[1728 + c]);
      const float y1 = (yp[c + 1] + yp[577 + c]) + (yp[1153 + c] + yp[1729 + c]);
      st_granule(ws + SY_PART + (fs * 576 + c) * 8, __float_as_uint(y0));
      st_granule(ws + SY_PART + (fs * 576 + c + 1) * 8, __float_as_uint(y1));
    }
  } else {
    const int c0 = (b - 144) * 6;
    LAS float* pp = sc + 3072;
    u64 hh = 0;
    if (w == 7) {
      const char* src = ws + SY_CH + (wpanel * 288 + (c0 >> 1) + min(lane, 2)) * 8;
      for (unsigned spin = 0; spin < SPIN_CAP; ++spin) {
        hh = ld_u64(src);
        if (__all((unsigned)(hh >> 32) == TAG)) break;
        __builtin_amdgcn_s_sleep(1);
      }
      if (lane < 48) {
#pragma unroll
        for (int c = 0; c < 6; ++c) pp[c * 192 + 144 + lane] = 0.f;
      }
      if (lane < 3) *(LAS unsigned*)(sc + 4224 + lane) = (unsigned)hh;
    } else {
      const int t2 = min(tid, 431), p = t2 / 3, c2 = t2 - p * 3;
      const char* src = ws + SY_PART + (p * 576 + c0 + c2 * 2) * 8;
      u64 a = 0, c = 0;
      for (unsigned spin = 0; spin < SPIN_CAP; ++spin) {
        a = ld_u64(src); c = ld_u64(src + 8);
        if (__all((unsigned)(a >> 32) == TAG && (unsigned)(c >> 32) == TAG)) break;
        __builtin_amdgcn_s_sleep(1);
      }
      pp[(c2 * 2) * 192 + p] = __uint_as_float((unsigned)a); pp[(c2 * 2 + 1) * 192 + p] = __uint_as_float((unsigned)c);
    }
    LDS_BAR();
    STAMP(13);
    if (w < 3) {
      float r = 0.f;
#pragma unroll
      for (int i = 0; i < 9; ++i) r = fmaf(xs[i * 64 + lane], wrs[i * 64 + lane], r);
      const float y0 = sum64(pp[(2 * w) * 192 + lane] + pp[(2 * w) * 192 + 64 + lane] + pp[(2 * w) * 192 + 128 + lane]);
      const float y1 = sum64(pp[(2 * w + 1) * 192 + lane] + pp[(2 * w + 1) * 192 + 64 + lane] + pp[(2 * w + 1) * 192 + 128 + lane]);
      r = rbf(sum64(r));
      const float sig = rbf(1.f / (1.f + __expf(-r)));
      const unsigned hp = *(LAS unsigned*)(sc + 4224 + w);
      const float r0 = bflo(hp) + rbf(xs[c0 + 2 * w] + rbf(rbf(y0) * sig));
      const float r1 = bfhi(hp) + rbf(xs[c0 + 2 * w + 1] + rbf(rbf(y1) * sig));
      if (lane == 0) *(unsigned*)(out + (size_t)sel * 1152 + (c0 + 2 * w) * 2) = cvtpk(r0, r1);
      if (b == 144 && tid == 0) st_u64(ws + WS_EPOCH, tag);
    }
  }
  STAMP_FLUSH(0);
}

extern "C" void kernel_launch(void* const* d_in, const int* in_sizes, int n_in, void* d_out, int out_size, void* d_ws,
                              size_t ws_size, hipStream_t stream) {
  (void)hipFuncSetAttribute((const void*)k_block, hipFuncAttributeMaxDynamicSharedMemorySize, LDS_BYTES);
  k_block<<<256, 512, LDS_BYTES, stream>>>((const char*)d_in[0], (const char*)d_in[1], (const char*)d_in[2], (const char*)d_in[4],
                                           (const char*)d_in[5], (const char*)d_in[6], (const char*)d_in[7], (char*)d_out, (char*)d_ws);
}
```

```cpp
#include <hip/hip_runtime.h>
#include <hip/hip_bf16.h>

typedef __attribute__((ext_vector_type(8))) short bf16x8;
typedef __attribute__((ext_vector_type(4))) short s16x4;
typedef __attribute__((ext_vector_type(16))) float f32x16;
typedef __attribute__((ext_vector_type(4))) float f32x4;
typedef __attribute__((ext_vector_type(4))) unsigned u32x4;
typedef __attribute__((ext_vector_type(2))) unsigned u32x2;
typedef __attribute__((ext_vector_type(2))) __bf16 b2_t;
typedef unsigned long long u64;
#define LAS __attribute__((address_space(3)))
#define GAS __attribute__((address_space(1)))
typedef LAS const char* lds_cptr;
typedef LAS char* lds_ptr;
typedef GAS u64 gu64;
typedef GAS unsigned gu32;

constexpr int S_ = 2048, D_ = 576, H_ = 32, F_ = 4608;
constexpr int CHUNK_B = 19456;
constexpr int HEAD_B = 8 * CHUNK_B;
constexpr int WS_EPOCH = 0;
constexpr int WS_FLAG1 = 256;
constexpr int WS_KV = 8192;
constexpr int WS_Q1 = WS_KV + H_ * HEAD_B;
constexpr int WS_Q2 = WS_Q1 + H_ * 65536;
constexpr int WS_SENT = WS_Q2 + H_ * 8192;
constexpr int WS_O1 = WS_SENT;
constexpr int WS_O2 = WS_O1 + H_ * 65536;
constexpr int SY_STATS = WS_O2 + S_ * 128;
constexpr int SY_BEST = SY_STATS + 128 * 4096;
constexpr int SY_CHN = SY_BEST + 512;
constexpr int SY_CH = SY_CHN + 128 * 1152;
constexpr int SY_PART = SY_CH + 128 * 1152;
constexpr int SY_END = SY_PART + 144 * 2304;
static_assert(SY_END - WS_SENT <= 256 * 16384, "sentinel area");
constexpr unsigned SENT = 0xFFFFFFFFu;

#define MFMA32(a, b, c) __builtin_amdgcn_mfma_f32_32x32x16_bf16(a, b, c, 0, 0, 0)
#define MFMA32K8(a, b, c) __builtin_amdgcn_mfma_f32_32x32x8bf16_1k(a, b, c, 0, 0, 0)
#define MFMA16(a, b, c) __builtin_amdgcn_mfma_f32_16x16x32_bf16(a, b, c, 0, 0, 0)
#define WAIT_VM(N) asm volatile("s_waitcnt vmcnt(" #N ")" ::: "memory")
#define WAIT_BAR(N) asm volatile("s_waitcnt vmcnt(" #N ") lgkmcnt(0)\n\ts_barrier" ::: "memory")
#define LDS_BAR() asm volatile("s_waitcnt lgkmcnt(0)\n\ts_barrier" ::: "memory")
#define PIN(x) asm volatile("" : "+v"(x))
#define SB() __builtin_amdgcn_sched_barrier(0)

__device__ __forceinline__ unsigned cvtpk(float lo, float hi) {
  typedef __attribute__((ext_vector_type(2))) float f2_t;
  return __builtin_bit_cast(unsigned, __builtin_convertvector((f2_t{lo, hi}), b2_t));
}
__device__ __forceinline__ float bflo(unsigned u) { return __uint_as_float(u << 16); }
__device__ __forceinline__ float bfhi(unsigned u) { return __uint_as_float(u & 0xffff0000u); }
__device__ __forceinline__ float rbf(float x) { return bflo(cvtpk(x, x)); }
__device__ __forceinline__ float dot2(unsigned a, unsigned b, float c) {
  return __builtin_amdgcn_fdot2_f32_bf16(__builtin_bit_cast(b2_t, a), __builtin_bit_cast(b2_t, b), c, false);
}
__device__ __forceinline__ void glds16(const void* sbase, unsigned voff, unsigned lds_dst) {
  unsigned sv;
  asm volatile("s_mov_b32 %0, m0\n\ts_mov_b32 m0, %3\n\ts_nop 0\n\tglobal_load_lds_dwordx4 %1, %2\n\ts_mov_b32 m0, %0"
               : "=&s"(sv) : "v"(voff), "s"(sbase), "s"(lds_dst) : "memory");
}
__device__ __forceinline__ u32x4 gload16(const void* sbase, unsigned voff) {
  u32x4 r; asm volatile("global_load_dwordx4 %0, %1, %2" : "=v"(r) : "v"(voff), "s"(sbase) : "memory"); return r;
}
__device__ __forceinline__ u32x2 gload8(const void* sbase, unsigned voff) {
  u32x2 r; asm volatile("global_load_dwordx2 %0, %1, %2" : "=v"(r) : "v"(voff), "s"(sbase) : "memory"); return r;
}
__device__ __forceinline__ u32x4 gload16s(const void* sbase, unsigned voff) {
  u32x4 r; asm volatile("global_load_dwordx4 %0, %1, %2 sc1" : "=v"(r) : "v"(voff), "s"(sbase) : "memory"); return r;
}
__device__ __forceinline__ u32x2 gload8s(const void* sbase, unsigned voff) {
  u32x2 r; asm volatile("global_load_dwordx2 %0, %1, %2 sc1" : "=v"(r) : "v"(voff), "s"(sbase) : "memory"); return r;
}
__device__ __forceinline__ unsigned gload4s(const void* sbase, unsigned voff) {
  unsigned r; asm volatile("global_load_dword %0, %1, %2 sc1" : "=v"(r) : "v"(voff), "s"(sbase) : "memory"); return r;
}
__device__ __forceinline__ void gstore16s(void* sbase, unsigned voff, u32x4 v) {
  asm volatile("global_store_dwordx4 %0, %1, %2 sc1\n\ts_nop 1" :: "v"(voff), "v"(v), "s"(sbase) : "memory");
}
__device__ __forceinline__ void gstore4s(void* sbase, unsigned voff, unsigned v) {
  asm volatile("global_store_dword %0, %1, %2 sc1" :: "v"(voff), "v"(v), "s"(sbase) : "memory");
}
__device__ __forceinline__ void gstore8s(void* sbase, unsigned voff, u32x2 v) {
  asm volatile("global_store_dwordx2 %0, %1, %2 sc1" :: "v"(voff), "v"(v), "s"(sbase) : "memory");
}
__device__ __forceinline__ unsigned gload4(const void* sbase, unsigned voff) {
  unsigned r; asm volatile("global_load_dword %0, %1, %2" : "=v"(r) : "v"(voff), "s"(sbase) : "memory"); return r;
}
__device__ __forceinline__ unsigned nsent(unsigned v) { return min(v, SENT - 1u); }
__device__ __forceinline__ unsigned mx4(u32x4 v) { return max(max(v[0], v[1]), max(v[2], v[3])); }
__device__ __forceinline__ bf16x8 lds128(lds_cptr p) { return *(const LAS bf16x8*)p; }
__device__ __forceinline__ s16x4 ldstr(lds_cptr p) { return __builtin_amdgcn_ds_read_tr16_b64_v4i16((LAS s16x4*)p); }
__device__ __forceinline__ bf16x8 cat8(s16x4 a, s16x4 b) { return __builtin_shufflevector(a, b, 0, 1, 2, 3, 4, 5, 6, 7); }
__device__ __forceinline__ void st_u64(char* p, u64 v) { __hip_atomic_store((gu64*)p, v, __ATOMIC_RELAXED, __HIP_MEMORY_SCOPE_AGENT); }
__device__ __forceinline__ u64 ld_u64(const char* p) { return __hip_atomic_load((gu64*)p, __ATOMIC_RELAXED, __HIP_MEMORY_SCOPE_AGENT); }
constexpr unsigned SPIN_CAP = 1u << 18;
template <int CTRL> __device__ __forceinline__ unsigned dppu(unsigned x) { return (unsigned)__builtin_amdgcn_mov_dpp((int)x, CTRL, 0xf, 0xf, true); }
template <int CTRL> __device__ __forceinline__ float dppf(float x) { return __uint_as_float(dppu<CTRL>(__float_as_uint(x))); }
__device__ __forceinline__ float sum_x16(float x) { auto s = __builtin_amdgcn_permlane16_swap(__float_as_uint(x), __float_as_uint(x), false, false); return __uint_as_float(s[0]) + __uint_as_float(s[1]); }
__device__ __forceinline__ float sum_x32(float x) { auto s = __builtin_amdgcn_permlane32_swap(__float_as_uint(x), __float_as_uint(x), false, false); return __uint_as_float(s[0]) + __uint_as_float(s[1]); }
__device__ __forceinline__ float sum8(float x) { x += dppf<0xB1>(x); x += dppf<0x4E>(x); x += dppf<0x141>(x); return x; }
__device__ __forceinline__ float sum64(float x) { x = sum8(x); x += dppf<0x140>(x); return sum_x32(sum_x16(x)); }
__device__ __forceinline__ unsigned umax64(unsigned x) {
  x = max(x, dppu<0xB1>(x)); x = max(x, dppu<0x4E>(x)); x = max(x, dppu<0x141>(x)); x = max(x, dppu<0x140>(x));
  auto s = __builtin_amdgcn_permlane16_swap(x, x, false, false); x = max(s[0], s[1]);
  auto t = __builtin_amdgcn_permlane32_swap(x, x, false, false); return max(t[0], t[1]);
}
#define STAMP_DECL
#define STAMP(i)
#define STAMP_FLUSH(kid)

constexpr int P1_APITCH = 1168;
constexpr int P1_XOFF = 64 * P1_APITCH;
constexpr int LDS_BYTES = 163840;
constexpr int P1_WIN = 6;

struct AttnState { f32x16 sA, oA, oB; u32x4 pend; bf16x8 v1p, k1; s16x4 k2; };
#define EXP2(x) __builtin_amdgcn_exp2f(x)
#define G4(P, i, S, r) do { P[i] = cvtpk(EXP2(S[r]), EXP2(S[(r) + 1])); P[(i) + 1] = cvtpk(EXP2(S[(r) + 2]), EXP2(S[(r) + 3])); } while (0)
__device__ __forceinline__ s16x4 ldk2(lds_cptr p) { return __builtin_bit_cast(s16x4, u32x2{*(const LAS unsigned*)p, 0u}); }
__device__ __forceinline__ bf16x8 ldv(lds_cptr p) { return cat8(ldstr(p), ldstr(p + 320)); }

template <class HOOK>
__device__ __forceinline__ void attn8(AttnState& st, lds_cptr kp, lds_cptr k2p, lds_cptr vp, lds_cptr kn, lds_cptr k2n,
                                      bf16x8 qa1, s16x4 qa2, bf16x8 qb1, s16x4 qb2, HOOK hook) {
#pragma unroll
  for (int t = 0; t < 8; ++t) {
    u32x4 p0, p1; f32x16 sB;
    const bf16x8 v0 = ldv(vp + t * 1280), v1 = ldv(vp + t * 1280 + 640);
    const bf16x8 k1n = lds128(t < 7 ? kp + (t + 1) * 1024 : kn);
    const s16x4 k2nx = ldk2(t < 7 ? k2p + (t + 1) * 128 : k2n);
    hook(t);
    SB();
    st.oB = MFMA32(st.v1p, __builtin_bit_cast(bf16x8, st.pend), st.oB); SB();
    G4(p0, 0, st.sA, 0); SB();
    sB = MFMA32(st.k1, qb1, f32x16{}); SB();
    G4(p0, 2, st.sA, 4); SB();
    sB = MFMA32K8(st.k2, qb2, sB); SB();
    G4(p1, 0, st.sA, 8); SB();
    st.oA = MFMA32(v0, __builtin_bit_cast(bf16x8, p0), st.oA); SB();
    G4(p1, 2, st.sA, 12); SB();
    st.oA = MFMA32(v1, __builtin_bit_cast(bf16x8, p1), st.oA); SB();
    G4(p0, 0, sB, 0); SB();
    st.sA = MFMA32(k1n, qa1, f32x16{}); SB();
    G4(p0, 2, sB, 4); SB();
    st.sA = MFMA32K8(k2nx, qa2, st.sA); SB();
    G4(p1, 0, sB, 8); SB();
    st.oB = MFMA32(v0, __builtin_bit_cast(bf16x8, p0), st.oB); SB();
    G4(p1, 2, sB, 12); SB();
    st.pend = p1; st.v1p = v1; st.k1 = k1n; st.k2 = k2nx;
  }
  PIN(st.oA); PIN(st.oB); PIN(st.pend); PIN(st.sA);
}
__device__ __forceinline__ int kv_goff(int u) {
  const int slot = u / 19, pc = u - slot * 19;
  return ((slot & 1) * 4 + (slot >> 1)) * CHUNK_B + pc * 1024;
}
constexpr int P2_XOFF = 82944;
constexpr int P3_XOFF = 116736;
constexpr int FB_OFF = 137216;
constexpr int P3_SOFF = 147456;

__global__ void __launch_bounds__(512, 2) k_block(const char* __restrict__ x, const char* __restrict__ wqkv, const char* __restrict__ wo,
                                                  const char* __restrict__ wr, const char* __restrict__ wg, const char* __restrict__ wu,
                                                  const char* __restrict__ wd, char* __restrict__ out, char* __restrict__ ws) {
  extern __shared__ __attribute__((aligned(16))) char smem[];
  const int tid = threadIdx.x, lane = tid & 63, w = __builtin_amdgcn_readfirstlane(tid >> 6);
  const int b = blockIdx.x, xc = b & 7, sl = b >> 3;
  const unsigned lds0 = (unsigned)(uintptr_t)smem;
  const int h = lane >> 5, r32 = lane & 31;
  STAMP_DECL STAMP(0);
  u64 tag;
  {
    const int j = 4 * (xc >> 2) + (sl >> 3), rb = 8 * (xc & 3) + (sl & 7);
    const int g4 = w & 3, kh = w >> 2;
    const int row = tid >> 3, j8 = tid & 7;
    u32x4 xr[9];
    {
      const char* xb = x + (size_t)rb * 64 * 1152;
      const unsigned vo = row * 1152 + j8 * 16;
#pragma unroll
      for (int i = 0; i < 9; ++i) xr[i] = gload16(xb, vo + i * 128);
    }
    u32x2 ep = gload8s(ws, WS_EPOCH);
    unsigned voff[8];
    {
      const int q = r32 >> 3, pi = r32 & 7;
      const int cb = q < 3 ? 36 * q + 4 * pi : 36 * min(pi, 2) + 32;
#pragma unroll
      for (int i = 0; i < 8; ++i) voff[i] = (8 * h + i) * 3456 + j * 432 + g4 * 108 + cb;
    }
    const int rot = ((sl & 7) * 9) >> 2;
    const char* wsrc = wqkv + (size_t)kh * (288 * 3456);
    unsigned wv[18][8];
    auto wload = [&](int s) {
      int ks = s + rot; ks = ks >= 18 ? ks - 18 : ks;
#pragma unroll
      for (int i = 0; i < 8; ++i) wv[s][i] = gload4(wsrc + ks * (16 * 3456), voff[i]);
    };
    wload(0); wload(1); wload(2);
    gstore16s(ws, WS_SENT + b * 16384 + tid * 16, u32x4{SENT, SENT, SENT, SENT});
    gstore16s(ws, WS_SENT + b * 16384 + 8192 + tid * 16, u32x4{SENT, SENT, SENT, SENT});
    WAIT_VM(26);
#pragma unroll
    for (int i = 0; i < 9; ++i) PIN(xr[i]);
    PIN(ep);
    tag = (((u64)ep[1] << 32) | ep[0]) * 6364136223846793005ull + 1442695040888963407ull;
    STAMP(1);
    {
      float s = 0.f, q = 0.f;
#pragma unroll
      for (int i = 0; i < 9; ++i)
#pragma unroll
        for (int e = 0; e < 4; ++e) { s = dot2(xr[i][e], 0x3F803F80u, s); q = dot2(xr[i][e], xr[i][e], q); }
      wload(3);
      s = sum8(s); q = sum8(q);
      const float m = s * (1.f / 576.f);
      const float var = fmaxf(fmaf(q, 1.f / 576.f, -m * m), 0.f);
      const float rinv = 1.f / rbf(sqrtf(rbf(rbf(var) + 1e-5f)));
      const float nb = -rbf(m) * rinv;
      const lds_ptr ap = (lds_ptr)smem + row * P1_APITCH + j8 * 16;
#pragma unroll
      for (int i = 0; i < 9; ++i) {
        u32x4 o;
#pragma unroll
        for (int e = 0; e < 4; ++e) o[e] = cvtpk(fmaf(bflo(xr[i][e]), rinv, nb), fmaf(bfhi(xr[i][e]), rinv, nb));
        *(LAS u32x4*)(ap + i * 128) = o;
        if (i == 2) wload(4);
        if (i == 5) wload(5);
      }
    }
    LDS_BAR();
    STAMP(2);
    f32x16 acc[2][2];
#pragma unroll
    for (int i = 0; i < 4; ++i) acc[i >> 1][i & 1] = f32x16{};
    {
      const lds_cptr a0 = (lds_cptr)smem + (kh * 32 + r32) * P1_APITCH + kh * 576 + h * 16;
      const lds_cptr a1 = (lds_cptr)smem + ((kh ^ 1) * 32 + r32) * P1_APITCH + kh * 576 + h * 16;
      bf16x8 xa = lds128(a0 + rot * 32), xb = lds128(a1 + rot * 32);
#pragma unroll
      for (int s = 0; s < 18; ++s) {
        bf16x8 xan = xa, xbn = xb;
        if (s < 17) { int ks = s + 1 + rot; ks = ks >= 18 ? ks - 18 : ks; xan = lds128(a0 + ks * 32); xbn = lds128(a1 + ks * 32); }
        if (s <= 12) WAIT_VM(40); else if (s == 13) WAIT_VM(32); else if (s == 14) WAIT_VM(24); else if (s == 15) WAIT_VM(16);
        else if (s == 16) WAIT_VM(8); else WAIT_VM(0);
        u32x4 f0, f1;
#pragma unroll
        for (int i = 0; i < 4; ++i) {
          PIN(wv[s][2 * i]); PIN(wv[s][2 * i + 1]);
          f0[i] = __builtin_amdgcn_perm(wv[s][2 * i + 1], wv[s][2 * i], 0x05040100u);
          f1[i] = __builtin_amdgcn_perm(wv[s][2 * i + 1], wv[s][2 * i], 0x07060302u);
        }
        PIN(f0); PIN(f1);
        if (s + P1_WIN < 18) wload(s + P1_WIN);
        acc[0][0] = MFMA32(__builtin_bit_cast(bf16x8, f0), xa, acc[0][0]);
        acc[1][0] = MFMA32(__builtin_bit_cast(bf16x8, f1), xa, acc[1][0]);
        acc[0][1] = MFMA32(__builtin_bit_cast(bf16x8, f0), xb, acc[0][1]);
        acc[1][1] = MFMA32(__builtin_bit_cast(bf16x8, f1), xb, acc[1][1]);
        xa = xan; xb = xbn;
      }
    }
    STAMP(3);
    {
      LAS f32x4* xw = (LAS f32x4*)(smem + P1_XOFF) + w * 512 + lane;
      LAS f32x4* xr_ = (LAS f32x4*)(smem + P1_XOFF) + (w ^ 4) * 512 + lane;
#pragma unroll
      for (int i = 0; i < 8; ++i) xw[i * 64] = f32x4{acc[i >> 2][1][4 * (i & 3)], acc[i >> 2][1][4 * (i & 3) + 1], acc[i >> 2][1][4 * (i & 3) + 2], acc[i >> 2][1][4 * (i & 3) + 3]};
      LDS_BAR();
#pragma unroll
      for (int i = 0; i < 8; ++i) {
        const f32x4 t = xr_[i * 64];
#pragma unroll
        for (int e = 0; e < 4; ++e) acc[i >> 2][0][4 * (i & 3) + e] += t[e];
      }
    }
    constexpr float QS = 0.23570226039551584f * 1.4426950408889634f;
    const int tile = 2 * rb + kh;
    const int kvt = WS_KV + (tile >> 3) * CHUNK_B;
#pragma unroll
    for (int q = 0; q < 3; ++q) {
      const int sidx = 12 * j + 3 * g4 + q, ty = sidx >> 5, hd = sidx & 31;
      const float sc = ty == 0 ? QS : 1.f;
      u32x4 o;
#pragma unroll
      for (int e = 0; e < 4; ++e) o[e] = cvtpk(acc[0][0][4 * q + e] * sc, acc[1][0][4 * q + e] * sc);
      const unsigned o2 = cvtpk(acc[0][0][12 + q] * sc, acc[1][0][12 + q] * sc);
      if (ty == 2) {
        const int base = kvt + hd * HEAD_B + 9216 + (tile & 7) * 1280 + r32 * 40;
        gstore16s(ws, base + h * 16, o);
        if (!h) gstore8s(ws, base + 32, u32x2{o2, 0x00003F80u});
      } else {
        const int b1 = ty ? kvt + hd * HEAD_B + (tile & 7) * 1024 : WS_Q1 + hd * 65536 + tile * 1024;
        const int b2 = ty ? kvt + hd * HEAD_B + 8192 + (tile & 7) * 128 : WS_Q2 + hd * 8192 + tile * 128;
        gstore16s(ws, b1 + lane * 16, o);
        if (!h) gstore4s(ws, b2 + r32 * 4, o2);
      }
    }
    WAIT_BAR(0);
    if (tid == 0) st_u64(ws + WS_FLAG1 + (j * 32 + rb) * 8, tag);
    STAMP(4);
  }
  const int panel = 4 * xc + (sl & 3), cg = sl >> 2;
  const int mt = w & 3, kq = w >> 2, g = lane >> 4, l16 = lane & 15;
  const int nnt = kq ? 2 : 3, nt0 = kq ? 3 : 0;
  const int tok = panel * 64 + mt * 16 + l16;
  const bool ffn = b < 144;
  const int fs = ((b & 7) + 8 * (b >> 4)) * 2 + ((b >> 3) & 1);
  u32x2 xres[3], wrr[3], gw[9], uw[9];
  unsigned wrow[5];
  u32x4 dw[8];
  {
    const int head = 4 * xc + (sl & 3), qp = sl >> 2;
    const int qpair = w & 3, kh = w >> 2;
    if (w == 0) {
      for (unsigned spin = 0; spin < SPIN_CAP; ++spin) {
        u32x4 fa = gload16s(ws, WS_FLAG1 + lane * 32), fb = gload16s(ws, WS_FLAG1 + lane * 32 + 16);
        WAIT_VM(0); PIN(fa); PIN(fb);
        const unsigned t0 = (unsigned)tag, t1 = (unsigned)(tag >> 32);
        if (__all(fa[0] == t0 && fa[1] == t1 && fa[2] == t0 && fa[3] == t1 && fb[0] == t0 && fb[1] == t1 && fb[2] == t0 && fb[3] == t1)) break;
      }
    }
    LDS_BAR();
    STAMP(5);
    const char* img = ws + WS_KV + (size_t)head * HEAD_B;
    const int qt = qp * 8 + 2 * qpair;
    u32x4 kv[19];
#pragma unroll
    for (int i = 0; i < 5; ++i) kv[i] = gload16s(img + kv_goff(8 * i + w), lane * 16);
    u32x4 q1a = gload16s(ws, WS_Q1 + head * 65536 + qt * 1024 + lane * 16);
    u32x4 q1b = gload16s(ws, WS_Q1 + head * 65536 + qt * 1024 + 1024 + lane * 16);
    unsigned q2a = gload4s(ws, WS_Q2 + head * 8192 + qt * 128 + r32 * 4);
    unsigned q2b = gload4s(ws, WS_Q2 + head * 8192 + qt * 128 + 128 + r32 * 4);
#pragma unroll
    for (int i = 5; i < 8; ++i) kv[i] = gload16s(img + kv_goff(8 * i + w), lane * 16);
    const lds_ptr kdst = (lds_ptr)smem + w * 1024 + lane * 16;
    WAIT_VM(3);
#pragma unroll
    for (int i = 0; i < 5; ++i) { PIN(kv[i]); *(LAS u32x4*)(kdst + i * 8192) = kv[i]; }
    PIN(q1a); PIN(q1b); PIN(q2a); PIN(q2b);
    const bf16x8 qa1 = __builtin_bit_cast(bf16x8, q1a), qb1 = __builtin_bit_cast(bf16x8, q1b);
    const s16x4 qa2 = __builtin_bit_cast(s16x4, u32x2{h ? 0u : q2a, 0u}), qb2 = __builtin_bit_cast(s16x4, u32x2{h ? 0u : q2b, 0u});
    const int vlo = (4 * h + (l16 >> 2)) * 40 + ((lane & 16) ? 32 : (lane & 3) * 8);
    const lds_cptr kp = (lds_cptr)smem + kh * CHUNK_B + lane * 16;
    const lds_cptr k2p = (lds_cptr)smem + kh * CHUNK_B + 8192 + r32 * 4;
    const lds_cptr vp = (lds_cptr)smem + kh * CHUNK_B + 9216 + vlo;
    LDS_BAR();
    STAMP(6);
    AttnState st;
    st.oA = f32x16{}; st.oB = f32x16{}; st.pend = u32x4{0, 0, 0, 0}; st.v1p = bf16x8{};
    st.k1 = lds128(kp); st.k2 = ldk2(k2p);
    st.sA = MFMA32K8(st.k2, qa2, MFMA32(st.k1, qa1, f32x16{}));
    attn8(st, kp, k2p, vp, kp, k2p, qa1, qa2, qb1, qb2, [&](int t) {
#pragma unroll
      for (int i = 8 + 3 * t; i < 11 + 3 * t; ++i) if (i < 19) kv[i] = gload16s(img + kv_goff(8 * i + w), lane * 16);
    });
    WAIT_VM(0);
#pragma unroll
    for (int i = 5; i < 19; ++i) { PIN(kv[i]); *(LAS u32x4*)(kdst + i * 8192) = kv[i]; }
    LDS_BAR();
    st.k1 = lds128(kp + 2 * CHUNK_B); st.k2 = ldk2(k2p + 2 * CHUNK_B);
    st.sA = MFMA32K8(st.k2, qa2, MFMA32(st.k1, qa1, f32x16{}));
    const unsigned go = ffn ? (tid >> 3) * (F_ * 2) + fs * 64 + (tid & 7) * 8 : 0u;
    attn8(st, kp + 2 * CHUNK_B, k2p + 2 * CHUNK_B, vp + 2 * CHUNK_B, kp + 4 * CHUNK_B, k2p + 4 * CHUNK_B, qa1, qa2, qb1, qb2, [&](int t) {
      gw[t] = gload8(wg + t * 64 * (F_ * 2), go);
      if (t == 7) gw[8] = gload8(wg + 8 * 64 * (F_ * 2), go);
      uw[t] = gload8(wu + t * 64 * (F_ * 2), go);
      if (t == 7) uw[8] = gload8(wu + 8 * 64 * (F_ * 2), go);
    });
    const unsigned dn = ffn ? (fs * 32 + g * 8) * 1152 + w * 144 + min(l16, 8) * 16 : 0u;
    attn8(st, kp + 4 * CHUNK_B, k2p + 4 * CHUNK_B, vp + 4 * CHUNK_B, kp + 6 * CHUNK_B, k2p + 6 * CHUNK_B, qa1, qa2, qb1, qb2, [&](int t) {
      dw[t] = gload16(wd + t * 1152, dn);
      if (t < 3) {
        const int n = min((nt0 + t) * 16 + 4 * g, 68);
        xres[t] = gload8(x, tok * 1152 + (cg * 72 + n) * 2);
        wrr[t] = gload8(wr, (cg * 72 + n) * 2);
      } else {
        wrow[t - 3] = gload4(wr, min((t - 3) * 64 + lane, 287) * 4);
      }
    });
    LDS_BAR();
    attn8(st, kp + 6 * CHUNK_B, k2p + 6 * CHUNK_B, vp + 6 * CHUNK_B, kp + 6 * CHUNK_B, k2p + 6 * CHUNK_B, qa1, qa2, qb1, qb2, [&](int t) {
#pragma unroll
      for (int i = 3 * t; i < 3 * t + 3; ++i) {
        if (i < 11) {
          const int pc = i < 10 ? 8 * i + w : 80;
          const int ci = pc * 64 + lane;
          const int R = ci / 9, cc = ci - R * 9;
          glds16(wo, R * 1152 + cg * 144 + cc * 16, lds0 + pc * 1024);
        }
      }
    });
    st.oB = MFMA32(st.v1p, __builtin_bit_cast(bf16x8, st.pend), st.oB);
    STAMP(7);
    LAS f32x4* xw = (LAS f32x4*)(smem + P2_XOFF) + (qpair * 2 + kh) * 192 + lane;
    LAS f32x4* xr_ = (LAS f32x4*)(smem + P2_XOFF) + (qpair * 2 + (kh ^ 1)) * 192 + lane;
    f32x16 o = kh ? st.oB : st.oA;
    {
      const f32x16 give = kh ? st.oA : st.oB;
#pragma unroll
      for (int r = 0; r < 3; ++r) xw[r * 64] = f32x4{give[4 * r], give[4 * r + 1], give[4 * r + 2], give[4 * r + 3]};
    }
    WAIT_BAR(0);
#pragma unroll
    for (int r = 0; r < 3; ++r) {
      const f32x4 t = xr_[r * 64];
#pragma unroll
      for (int e = 0; e < 4; ++e) o[4 * r + e] += t[e];
    }
    float l;
    {
      auto sw = __builtin_amdgcn_permlane32_swap(__float_as_uint(o[10]), __float_as_uint(o[10]), false, false);
      l = __uint_as_float(sw[0]);
    }
    bool ok = (l > 8.6736174e-19f) && (l < 1.1529215e18f);
#pragma unroll
    for (int r = 0; r < 10; ++r) ok = ok && ((h && r >= 8) || fabsf(o[r]) < 3.0e38f);
    if (!__all(ok)) {
      const bf16x8 qm1 = kh ? qb1 : qa1; const s16x4 qm2 = kh ? qb2 : qa2;
      const lds_ptr fb = (lds_ptr)smem + FB_OFF + w * 1280;
      float mx = 0.f;
#pragma unroll 1
      for (int pass = 0; pass < 2; ++pass) {
        f32x16 cin;
#pragma unroll
        for (int r = 0; r < 16; ++r) cin[r] = -mx;
        float m2 = -INFINITY;
        o = f32x16{};
#pragma unroll 1
        for (int kt = 0; kt < 64; ++kt) {
          const char* cb = img + (kt >> 3) * CHUNK_B;
          u32x4 k1 = gload16s(cb + (kt & 7) * 1024, lane * 16);
          unsigned k2 = gload4s(cb + 8192 + (kt & 7) * 128, r32 * 4);
          u32x4 va = gload16s(cb + 9216 + (kt & 7) * 1280, lane * 16), vb = gload16s(cb + 9216 + (kt & 7) * 1280 + 1024, l16 * 16);
          WAIT_VM(0); PIN(k1); PIN(k2); PIN(va); PIN(vb);
          *(LAS u32x4*)(fb + lane * 16) = va; *(LAS u32x4*)(fb + 1024 + l16 * 16) = vb;
          f32x16 s = MFMA32(__builtin_bit_cast(bf16x8, k1), qm1, cin);
          s = MFMA32K8(__builtin_bit_cast(s16x4, (u32x2{k2, 0u})), qm2, s);
#pragma unroll
          for (int r = 0; r < 16; ++r) m2 = fmaxf(m2, s[r]);
          u32x4 p0, p1;
          G4(p0, 0, s, 0); G4(p0, 2, s, 4); G4(p1, 0, s, 8); G4(p1, 2, s, 12);
          asm volatile("s_waitcnt lgkmcnt(0)" ::: "memory");
          o = MFMA32(ldv((lds_cptr)fb + vlo), __builtin_bit_cast(bf16x8, p0), o);
          o = MFMA32(ldv((lds_cptr)fb + vlo + 640), __builtin_bit_cast(bf16x8, p1), o);
          asm volatile("s_waitcnt lgkmcnt(0)" ::: "memory");
        }
        auto sw = __builtin_amdgcn_permlane32_swap(__float_as_uint(m2), __float_as_uint(m2), false, false);
        mx = fmaxf(__uint_as_float(sw[0]), __uint_as_float(sw[1]));
      }
      auto sw = __builtin_amdgcn_permlane32_swap(__float_as_uint(o[10]), __float_as_uint(o[10]), false, false);
      l = __uint_as_float(sw[0]);
    }
    const float rl = 1.f / l;
    u32x4 ov;
#pragma unroll
    for (int e = 0; e < 4; ++e) ov[e] = nsent(cvtpk(o[2 * e] * rl, o[2 * e + 1] * rl));
    gstore16s(ws, WS_O1 + head * 65536 + (qt + kh) * 1024 + lane * 16, ov);
    if (!h) gstore4s(ws, WS_O2 + ((qt + kh) * 32 + r32) * 128 + head * 4, nsent(cvtpk(o[8] * rl, o[9] * rl)));
    STAMP(8);
  }
  float wsum;
  {
#pragma unroll
    for (int i = 0; i < 5; ++i) PIN(wrow[i]);
    if (h) wrow[4] = 0u;
    float t = 0.f;
#pragma unroll
    for (int i = 0; i < 5; ++i) t = dot2(wrow[i], 0x3F803F80u, t);
    wsum = sum64(t);
  }
  f32x4 acc[5];
  {
    u32x4 of[9];
    const unsigned o1 = WS_O1 + (kq * 18 + (g >> 1)) * 65536 + (2 * panel + (mt >> 1)) * 1024 + (g & 1) * 512 + ((mt & 1) * 16 + l16) * 16;
    const unsigned o2 = WS_O2 + tok * 128 + g * 16;
    const unsigned a7 = kq ? o2 : o1 + 7 * 131072, a8 = kq ? o2 + 64 : o1 + 8 * 131072;
    for (unsigned spin = 0; spin < SPIN_CAP; ++spin) {
#pragma unroll
      for (int i = 0; i < 7; ++i) of[i] = gload16s(ws, o1 + i * 131072);
      of[7] = gload16s(ws, a7); of[8] = gload16s(ws, a8);
      WAIT_VM(0);
      unsigned m = 0;
#pragma unroll
      for (int i = 0; i < 9; ++i) { PIN(of[i]); m = max(m, mx4(of[i])); }
      if (__all(m != SENT)) break;
    }
    STAMP(9);
    const int q = l16 >> 2;
    const lds_cptr bp = (lds_cptr)smem + (kq * 9 * 36 + 18 * (g >> 1) + 4 * (g & 1) + q) * 144 + (lane & 3) * 8;
    const lds_cptr bp2 = (lds_cptr)smem + (18 * (4 * g + (q >> 1)) + 16 + (q & 1)) * 144 + (lane & 3) * 8;
#pragma unroll
    for (int i = 0; i < 5; ++i) acc[i] = f32x4{};
#pragma unroll
    for (int ks = 0; ks < 9; ++ks) {
      const bool sp = ks >= 7 && kq;
      const lds_cptr b0 = ks >= 7 ? (sp ? bp2 + (ks - 7) * (288 * 144) : bp + ks * (36 * 144)) : bp + ks * (36 * 144);
      const int d2 = sp ? 36 * 144 : 8 * 144;
#pragma unroll
      for (int i = 0; i < 5; ++i) {
        const bf16x8 wf = cat8(ldstr(b0 + i * 32), ldstr(b0 + d2 + i * 32));
        acc[i] = MFMA16(wf, __builtin_bit_cast(bf16x8, of[ks]), acc[i]);
      }
    }
    LAS f32x4* xw = (LAS f32x4*)(smem + P3_XOFF) + mt * 320 + lane;
    if (kq == 0) { xw[3 * 64] = acc[3]; xw[4 * 64] = acc[4]; } else { xw[0] = acc[0]; xw[64] = acc[1]; xw[128] = acc[2]; }
    LDS_BAR();
    if (kq == 0) { acc[0] += xw[0]; acc[1] += xw[64]; acc[2] += xw[128]; }
    else { acc[0] = acc[3] + xw[3 * 64]; acc[1] = acc[4] + xw[4 * 64]; acc[2] = f32x4{}; }
  }
#pragma unroll
  for (int i = 0; i < 3; ++i) { PIN(xres[i]); PIN(wrr[i]); }
  STAMP(10);
  const int rgp = panel * 4 + mt;
  float hv[3][4];
  unsigned key; float mb, rinv;
  {
    float s1 = 0.f, s2 = 0.f, s3 = 0.f;
#pragma unroll
    for (int i = 0; i < 3; ++i) {
      const bool valid = (i < nnt) && ((nt0 + i) * 16 + 4 * g < 72);
      const unsigned a01 = cvtpk(acc[i][0], acc[i][1]), a23 = cvtpk(acc[i][2], acc[i][3]);
      unsigned h01 = cvtpk(bflo(xres[i][0]) + bflo(a01), bfhi(xres[i][0]) + bfhi(a01));
      unsigned h23 = cvtpk(bflo(xres[i][1]) + bflo(a23), bfhi(xres[i][1]) + bfhi(a23));
      h01 = valid ? h01 : 0u; h23 = valid ? h23 : 0u;
      hv[i][0] = bflo(h01); hv[i][1] = bfhi(h01); hv[i][2] = bflo(h23); hv[i][3] = bfhi(h23);
      s1 += (hv[i][0] + hv[i][1]) + (hv[i][2] + hv[i][3]);
      s2 = fmaf(hv[i][0], hv[i][0], fmaf(hv[i][1], hv[i][1], fmaf(hv[i][2], hv[i][2], fmaf(hv[i][3], hv[i][3], s2))));
      s3 = fmaf(hv[i][0], bflo(wrr[i][0]), fmaf(hv[i][1], bfhi(wrr[i][0]), fmaf(hv[i][2], bflo(wrr[i][1]), fmaf(hv[i][3], bfhi(wrr[i][1]), s3))));
    }
    s1 = sum_x32(sum_x16(s1)); s2 = sum_x32(sum_x16(s2)); s3 = sum_x32(sum_x16(s3));
    if (g == 0) gstore16s(ws, SY_STATS + rgp * 4096 + (cg * 2 + kq) * 256 + l16 * 16,
                          u32x4{nsent(__float_as_uint(s1)), nsent(__float_as_uint(s2)), nsent(__float_as_uint(s3)), 0u});
    u32x4 sv[4];
    for (unsigned spin = 0; spin < SPIN_CAP; ++spin) {
#pragma unroll
      for (int i = 0; i < 4; ++i) sv[i] = gload16s(ws, SY_STATS + rgp * 4096 + i * 1024 + lane * 16);
      WAIT_VM(0);
#pragma unroll
      for (int i = 0; i < 4; ++i) PIN(sv[i]);
      if (__all(max(max(mx4(sv[0]), mx4(sv[1])), max(mx4(sv[2]), mx4(sv[3]))) != SENT)) break;
    }
    STAMP(11);
#define SUMC(c) sum_x32(sum_x16((__uint_as_float(sv[0][c]) + __uint_as_float(sv[1][c])) + (__uint_as_float(sv[2][c]) + __uint_as_float(sv[3][c]))))
    s1 = SUMC(0); s2 = SUMC(1); s3 = SUMC(2);
    const float m = s1 * (1.f / 576.f);
    mb = rbf(m);
    rinv = 1.f / rbf(sqrtf(rbf(rbf(fmaxf(fmaf(s2, 1.f / 576.f, -m * m), 0.f)) + 1e-5f)));
    const unsigned u = __float_as_uint((s3 - mb * wsum) * rinv);
    const unsigned ord = (u & 0x80000000u) ? ~u : (u | 0x80000000u);
    key = (min(ord, 0xFFFFF000u) & ~0x7FFu) | (unsigned)(2047 - tok);
    key = max(key, dppu<0xB1>(key)); key = max(key, dppu<0x4E>(key)); key = max(key, dppu<0x141>(key)); key = max(key, dppu<0x140>(key));
  }
  if (cg == 0 && kq == 0 && lane == 0) gstore4s(ws, SY_BEST + rgp * 4, key);
  const bool isbest = tok == 2047 - (int)(key & 0x7FFu);
  u32x2 ov[3];
#pragma unroll
  for (int i = 0; i < 3; ++i) {
    const bool valid = (i < nnt) && ((nt0 + i) * 16 + 4 * g < 72);
    const unsigned d01 = cvtpk(hv[i][0] - mb, hv[i][1] - mb), d23 = cvtpk(hv[i][2] - mb, hv[i][3] - mb);
    const unsigned n01 = cvtpk(bflo(d01) * rinv, bfhi(d01) * rinv), n23 = cvtpk(bflo(d23) * rinv, bfhi(d23) * rinv);
    ov[i] = u32x2{cvtpk(hv[i][0] + bflo(n01), hv[i][1] + bfhi(n01)), cvtpk(hv[i][2] + bflo(n23), hv[i][3] + bfhi(n23))};
    const int col = cg * 72 + (nt0 + i) * 16 + 4 * g;
    if (valid && isbest) {
      gstore8s(ws, SY_CHN + rgp * 1152 + col * 2, u32x2{nsent(n01), nsent(n23)});
      gstore8s(ws, SY_CH + rgp * 1152 + col * 2, u32x2{nsent(cvtpk(hv[i][0], hv[i][1])), nsent(cvtpk(hv[i][2], hv[i][3]))});
    }
  }
#pragma unroll
  for (int i = 0; i < 3; ++i) {
    const bool valid = (i < nnt) && ((nt0 + i) * 16 + 4 * g < 72);
    if (valid && !isbest) *(u32x2*)(out + (size_t)tok * 1152 + (cg * 72 + (nt0 + i) * 16 + 4 * g) * 2) = ov[i];
  }
  int sel;
  {
    u32x2 a;
    for (unsigned spin = 0; spin < SPIN_CAP; ++spin) {
      a = gload8s(ws, SY_BEST + lane * 8);
      WAIT_VM(0); PIN(a);
      if (__all(max(a[0], a[1]) != SENT)) break;
    }
    sel = __builtin_amdgcn_readfirstlane(2047 - (int)(umax64(max(a[0], a[1])) & 0x7FFu));
  }
  const int wgrp = sel >> 4;
  STAMP(12);
  if (ffn) {
    unsigned xv[9];
    for (unsigned spin = 0; spin < SPIN_CAP; ++spin) {
#pragma unroll
      for (int i = 0; i < 9; ++i) xv[i] = gload4s(ws, SY_CHN + wgrp * 1152 + (tid >> 4) * 4 + i * 128);
      WAIT_VM(0);
      unsigned m = 0;
#pragma unroll
      for (int i = 0; i < 9; ++i) { PIN(xv[i]); m = max(m, xv[i]); }
      if (__all(m != SENT)) break;
    }
#pragma unroll
    for (int i = 0; i < 9; ++i) { PIN(gw[i]); PIN(uw[i]); }
#pragma unroll
    for (int i = 0; i < 8; ++i) PIN(dw[i]);
    STAMP(13);
    float ga[4] = {0.f, 0.f, 0.f, 0.f}, ua[4] = {0.f, 0.f, 0.f, 0.f};
#pragma unroll
    for (int i = 0; i < 9; ++i) {
      const float xf = (lane & 8) ? bfhi(xv[i]) : bflo(xv[i]);
      ga[0] = fmaf(xf, bflo(gw[i][0]), ga[0]); ga[1] = fmaf(xf, bfhi(gw[i][0]), ga[1]); ga[2] = fmaf(xf, bflo(gw[i][1]), ga[2]); ga[3] = fmaf(xf, bfhi(gw[i][1]), ga[3]);
      ua[0] = fmaf(xf, bflo(uw[i][0]), ua[0]); ua[1] = fmaf(xf, bfhi(uw[i][0]), ua[1]); ua[2] = fmaf(xf, bflo(uw[i][1]), ua[2]); ua[3] = fmaf(xf, bfhi(uw[i][1]), ua[3]);
    }
    LAS float* gp = (LAS float*)(smem + P3_SOFF);
#pragma unroll
    for (int e = 0; e < 4; ++e) { ga[e] = sum_x32(sum_x16(ga[e])); ua[e] = sum_x32(sum_x16(ua[e])); }
    if (lane < 16) {
      *(LAS f32x4*)(gp + (w * 2 + (lane >> 3)) * 64 + (lane & 7) * 4) = f32x4{ga[0], ga[1], ga[2], ga[3]};
      *(LAS f32x4*)(gp + (w * 2 + (lane >> 3)) * 64 + 32 + (lane & 7) * 4) = f32x4{ua[0], ua[1], ua[2], ua[3]};
    }
    LDS_BAR();
    float av;
    {
      float s = 0.f;
#pragma unroll
      for (int i = 0; i < 16; ++i) s += gp[i * 64 + lane];
      s = rbf(s);
      auto sw = __builtin_amdgcn_permlane32_swap(__float_as_uint(s), __float_as_uint(s), false, false);
      const float gs = __uint_as_float(sw[0]), us = __uint_as_float(sw[1]);
      av = rbf(rbf(gs / (1.f + __expf(-gs))) * us);
    }
    float y[8] = {0.f, 0.f, 0.f, 0.f, 0.f, 0.f, 0.f, 0.f};
#pragma unroll
    for (int i = 0; i < 8; ++i) {
      const float a = __shfl(av, g * 8 + i);
#pragma unroll
      for (int e = 0; e < 4; ++e) { y[2 * e] = fmaf(a, bflo(dw[i][e]), y[2 * e]); y[2 * e + 1] = fmaf(a, bfhi(dw[i][e]), y[2 * e + 1]); }
    }
#pragma unroll
    for (int e = 0; e < 8; ++e) y[e] = sum_x32(sum_x16(y[e]));
    const float y0 = g == 0 ? y[0] : g == 1 ? y[2] : g == 2 ? y[4] : y[6], y1 = g == 0 ? y[1] : g == 1 ? y[3] : g == 2 ? y[5] : y[7];
    if (l16 < 9) gstore8s(ws, SY_PART + fs * 2304 + (w * 72 + l16 * 8 + g * 2) * 4, u32x2{nsent(__float_as_uint(y0)), nsent(__float_as_uint(y1))});
  }
  if (wgrp != rgp && isbest) {
#pragma unroll
    for (int i = 0; i < 3; ++i) {
      const bool valid = (i < nnt) && ((nt0 + i) * 16 + 4 * g < 72);
      if (valid) *(u32x2*)(out + (size_t)tok * 1152 + (cg * 72 + (nt0 + i) * 16 + 4 * g) * 2) = ov[i];
    }
  }
  if (!ffn && b < 240 && w < 3) {
    const int cp = (b - 144) * 3 + w;
    unsigned xv[5], hp, np;
    for (unsigned spin = 0; spin < SPIN_CAP; ++spin) {
#pragma unroll
      for (int i = 0; i < 5; ++i) xv[i] = gload4s(ws, SY_CHN + wgrp * 1152 + min(i * 64 + lane, 287) * 4);
      hp = gload4s(ws, SY_CH + wgrp * 1152 + cp * 4); np = gload4s(ws, SY_CHN + wgrp * 1152 + cp * 4);
      WAIT_VM(0);
      PIN(hp); PIN(np);
      unsigned m = max(hp, np);
#pragma unroll
      for (int i = 0; i < 5; ++i) { PIN(xv[i]); m = max(m, xv[i]); }
      if (__all(m != SENT)) break;
    }
    float r = 0.f;
#pragma unroll
    for (int i = 0; i < 5; ++i) r = fmaf(bflo(xv[i]), bflo(wrow[i]), fmaf(bfhi(xv[i]), bfhi(wrow[i]), r));
    r = rbf(sum64(r));
    const float sig = rbf(1.f / (1.f + __expf(-r)));
    STAMP(13);
    u32x2 pv[3];
    for (unsigned spin = 0; spin < SPIN_CAP; ++spin) {
#pragma unroll
      for (int i = 0; i < 3; ++i) pv[i] = gload8s(ws, SY_PART + min(i * 64 + lane, 143) * 2304 + cp * 8);
      WAIT_VM(0);
      unsigned m = 0;
#pragma unroll
      for (int i = 0; i < 3; ++i) { PIN(pv[i]); m = max(m, max(pv[i][0], pv[i][1])); }
      if (__all(m != SENT)) break;
    }
    const bool v2 = lane < 16;
    const float y0 = sum64(__uint_as_float(pv[0][0]) + __uint_as_float(pv[1][0]) + (v2 ? __uint_as_float(pv[2][0]) : 0.f));
    const float y1 = sum64(__uint_as_float(pv[0][1]) + __uint_as_float(pv[1][1]) + (v2 ? __uint_as_float(pv[2][1]) : 0.f));
    const float r0 = bflo(hp) + rbf(bflo(np) + rbf(rbf(y0) * sig));
    const float r1 = bfhi(hp) + rbf(bfhi(np) + rbf(rbf(y1) * sig));
    if (lane == 0) *(unsigned*)(out + (size_t)sel * 1152 + cp * 4) = cvtpk(r0, r1);
    if (b == 144 && tid == 0) st_u64(ws + WS_EPOCH, tag);
  }
  STAMP_FLUSH(0);
}

extern "C" void kernel_launch(void* const* d_in, const int* in_sizes, int n_in, void* d_out, int out_size, void* d_ws,
                              size_t ws_size, hipStream_t stream) {
  (void)hipFuncSetAttribute((const void*)k_block, hipFuncAttributeMaxDynamicSharedMemorySize, LDS_BYTES);
  k_block<<<256, 512, LDS_BYTES, stream>>>((const char*)d_in[0], (const char*)d_in[1], (const char*)d_in[2], (const char*)d_in[4],
                                           (const char*)d_in[5], (const char*)d_in[6], (const char*)d_in[7], (char*)d_out, (char*)d_ws);
}
```

```cpp
#include <hip/hip_runtime.h>
#include <hip/hip_bf16.h>

typedef __attribute__((ext_vector_type(8))) short bf16x8;
typedef __attribute__((ext_vector_type(4))) short s16x4;
typedef __attribute__((ext_vector_type(16))) float f32x16;
typedef __attribute__((ext_vector_type(4))) float f32x4;
typedef __attribute__((ext_vector_type(4))) unsigned u32x4;
typedef __attribute__((ext_vector_type(2))) unsigned u32x2;
typedef __attribute__((ext_vector_type(2))) __bf16 b2_t;
typedef unsigned long long u64;
#define LAS __attribute__((address_space(3)))
#define GAS __attribute__((address_space(1)))
typedef LAS const char* lds_cptr;
typedef LAS char* lds_ptr;
typedef GAS u64 gu64;
typedef GAS unsigned gu32;

constexpr int S_ = 2048, D_ = 576, H_ = 32, F_ = 4608;
constexpr int CHUNK_B = 19456;
constexpr int HEAD_B = 8 * CHUNK_B;
constexpr int WS_EPOCH = 0;
constexpr int WS_FLAG1 = 4096;
constexpr int WS_KV = WS_FLAG1 + 8 * 4096;
constexpr int WS_Q1 = WS_KV + H_ * HEAD_B;
constexpr int WS_Q2 = WS_Q1 + H_ * 65536;
constexpr int WS_SENT = WS_Q2 + H_ * 8192;
constexpr int WS_O1 = WS_SENT;
constexpr int WS_O2 = WS_O1 + H_ * 65536;
constexpr int SY_STATS = WS_O2 + S_ * 128;
constexpr int SY_BEST = SY_STATS + 128 * 4096;
constexpr int SY_CHN = SY_BEST + 16 * 4096;
constexpr int SY_CH = SY_CHN + 128 * 1152;
constexpr int SY_PART = SY_CH + 128 * 1152;
constexpr int SY_END = SY_PART + 144 * 2304;
static_assert(SY_END - WS_SENT <= 256 * 16384, "sentinel area");
constexpr unsigned SENT = 0xFFFFFFFFu;

#define MFMA32(a, b, c) __builtin_amdgcn_mfma_f32_32x32x16_bf16(a, b, c, 0, 0, 0)
#define MFMA32K8(a, b, c) __builtin_amdgcn_mfma_f32_32x32x8bf16_1k(a, b, c, 0, 0, 0)
#define MFMA16(a, b, c) __builtin_amdgcn_mfma_f32_16x16x32_bf16(a, b, c, 0, 0, 0)
#define WAIT_VM(N) asm volatile("s_waitcnt vmcnt(" #N ")" ::: "memory")
#define WAIT_BAR(N) asm volatile("s_waitcnt vmcnt(" #N ") lgkmcnt(0)\n\ts_barrier" ::: "memory")
#define LDS_BAR() asm volatile("s_waitcnt lgkmcnt(0)\n\ts_barrier" ::: "memory")
#define PIN(x) asm volatile("" : "+v"(x))
#define SB() __builtin_amdgcn_sched_barrier(0)

__device__ __forceinline__ unsigned cvtpk(float lo, float hi) {
  typedef __attribute__((ext_vector_type(2))) float f2_t;
  return __builtin_bit_cast(unsigned, __builtin_convertvector((f2_t{lo, hi}), b2_t));
}
__device__ __forceinline__ float bflo(unsigned u) { return __uint_as_float(u << 16); }
__device__ __forceinline__ float bfhi(unsigned u) { return __uint_as_float(u & 0xffff0000u); }
__device__ __forceinline__ float rbf(float x) { return bflo(cvtpk(x, x)); }
__device__ __forceinline__ float dot2(unsigned a, unsigned b, float c) {
  return __builtin_amdgcn_fdot2_f32_bf16(__builtin_bit_cast(b2_t, a), __builtin_bit_cast(b2_t, b), c, false);
}
__device__ __forceinline__ void glds16(const void* sbase, unsigned voff, unsigned lds_dst) {
  unsigned sv;
  asm volatile("s_mov_b32 %0, m0\n\ts_mov_b32 m0, %3\n\ts_nop 0\n\tglobal_load_lds_dwordx4 %1, %2\n\ts_mov_b32 m0, %0"
               : "=&s"(sv) : "v"(voff), "s"(sbase), "s"(lds_dst) : "memory");
}
__device__ __forceinline__ u32x4 gload16(const void* sbase, unsigned voff) {
  u32x4 r; asm volatile("global_load_dwordx4 %0, %1, %2" : "=v"(r) : "v"(voff), "s"(sbase) : "memory"); return r;
}
__device__ __forceinline__ u32x2 gload8(const void* sbase, unsigned voff) {
  u32x2 r; asm volatile("global_load_dwordx2 %0, %1, %2" : "=v"(r) : "v"(voff), "s"(sbase) : "memory"); return r;
}
__device__ __forceinline__ u32x4 gload16s(const void* sbase, unsigned voff) {
  u32x4 r; asm volatile("global_load_dwordx4 %0, %1, %2 sc1" : "=v"(r) : "v"(voff), "s"(sbase) : "memory"); return r;
}
__device__ __forceinline__ u32x2 gload8s(const void* sbase, unsigned voff) {
  u32x2 r; asm volatile("global_load_dwordx2 %0, %1, %2 sc1" : "=v"(r) : "v"(voff), "s"(sbase) : "memory"); return r;
}
__device__ __forceinline__ unsigned gload4s(const void* sbase, unsigned voff) {
  unsigned r; asm volatile("global_load_dword %0, %1, %2 sc1" : "=v"(r) : "v"(voff), "s"(sbase) : "memory"); return r;
}
__device__ __forceinline__ void gstore16s(void* sbase, unsigned voff, u32x4 v) {
  asm volatile("global_store_dwordx4 %0, %1, %2 sc1\n\ts_nop 1" :: "v"(voff), "v"(v), "s"(sbase) : "memory");
}
__device__ __forceinline__ void gstore4s(void* sbase, unsigned voff, unsigned v) {
  asm volatile("global_store_dword %0, %1, %2 sc1" :: "v"(voff), "v"(v), "s"(sbase) : "memory");
}
__device__ __forceinline__ void gstore8s(void* sbase, unsigned voff, u32x2 v) {
  asm volatile("global_store_dwordx2 %0, %1, %2 sc1" :: "v"(voff), "v"(v), "s"(sbase) : "memory");
}
__device__ __forceinline__ unsigned gload4(const void* sbase, unsigned voff) {
  unsigned r; asm volatile("global_load_dword %0, %1, %2" : "=v"(r) : "v"(voff), "s"(sbase) : "memory"); return r;
}
__device__ __forceinline__ unsigned nsent(unsigned v) { return min(v, SENT - 1u); }
__device__ __forceinline__ unsigned mx4(u32x4 v) { return max(max(v[0], v[1]), max(v[2], v[3])); }
#define PL(W, R, M, A) "v_cmp_eq_u32 vcc, -1, %[" M "]\n\ts_and_saveexec_b64 %[sv], vcc\n\tglobal_load_" W " %[" R "], %[" A "], %[b] sc1\n\ts_mov_b64 exec, %[sv]\n\t"
#define PADD(N) "v_add_u32 %[t], " N ", %[t]\n\t"
#define PEND "s_waitcnt vmcnt(0)"
__device__ __forceinline__ void poll_o(u32x4 (&r)[9], const void* sb, unsigned a0, unsigned a7, unsigned a8) {
  u64 sv;
  asm volatile(PL("dwordx4", "r0", "m0", "t") PADD("0x20000") PL("dwordx4", "r1", "m1", "t") PADD("0x20000") PL("dwordx4", "r2", "m2", "t") PADD("0x20000")
               PL("dwordx4", "r3", "m3", "t") PADD("0x20000") PL("dwordx4", "r4", "m4", "t") PADD("0x20000") PL("dwordx4", "r5", "m5", "t") PADD("0x20000")
               PL("dwordx4", "r6", "m6", "t") PL("dwordx4", "r7", "m7", "a7") PL("dwordx4", "r8", "m8", "a8") PEND
               : [r0] "+v"(r[0]), [r1] "+v"(r[1]), [r2] "+v"(r[2]), [r3] "+v"(r[3]), [r4] "+v"(r[4]), [r5] "+v"(r[5]), [r6] "+v"(r[6]),
                 [r7] "+v"(r[7]), [r8] "+v"(r[8]), [sv] "=&s"(sv), [t] "+v"(a0)
               : [m0] "v"(mx4(r[0])), [m1] "v"(mx4(r[1])), [m2] "v"(mx4(r[2])), [m3] "v"(mx4(r[3])), [m4] "v"(mx4(r[4])), [m5] "v"(mx4(r[5])),
                 [m6] "v"(mx4(r[6])), [m7] "v"(mx4(r[7])), [m8] "v"(mx4(r[8])), [a7] "v"(a7), [a8] "v"(a8), [b] "s"(sb) : "vcc", "memory");
}
__device__ __forceinline__ void poll_4x16(u32x4 (&r)[4], const void* sb, unsigned a0) {
  u64 sv;
  asm volatile(PL("dwordx4", "r0", "m0", "t") PADD("0x400") PL("dwordx4", "r1", "m1", "t") PADD("0x400") PL("dwordx4", "r2", "m2", "t") PADD("0x400")
               PL("dwordx4", "r3", "m3", "t") PEND
               : [r0] "+v"(r[0]), [r1] "+v"(r[1]), [r2] "+v"(r[2]), [r3] "+v"(r[3]), [sv] "=&s"(sv), [t] "+v"(a0)
               : [m0] "v"(mx4(r[0])), [m1] "v"(mx4(r[1])), [m2] "v"(mx4(r[2])), [m3] "v"(mx4(r[3])), [b] "s"(sb) : "vcc", "memory");
}
__device__ __forceinline__ void poll_8(u32x2& r, const void* sb, unsigned a0) {
  u64 sv;
  asm volatile(PL("dwordx2", "r0", "m0", "a0") PEND
               : [r0] "+v"(r), [sv] "=&s"(sv) : [m0] "v"(max(r[0], r[1])), [a0] "v"(a0), [b] "s"(sb) : "vcc", "memory");
}
__device__ __forceinline__ void poll_5x4(unsigned (&r)[5], const void* sb, unsigned a0, unsigned a4) {
  u64 sv;
  asm volatile(PL("dword", "r0", "r0", "t") PADD("0x100") PL("dword", "r1", "r1", "t") PADD("0x100") PL("dword", "r2", "r2", "t") PADD("0x100")
               PL("dword", "r3", "r3", "t") PL("dword", "r4", "r4", "a4") PEND
               : [r0] "+v"(r[0]), [r1] "+v"(r[1]), [r2] "+v"(r[2]), [r3] "+v"(r[3]), [r4] "+v"(r[4]), [sv] "=&s"(sv), [t] "+v"(a0)
               : [a4] "v"(a4), [b] "s"(sb) : "vcc", "memory");
}
__device__ __forceinline__ void poll_part(u32x2 (&r)[3], unsigned& hp, const void* sb, unsigned a0, unsigned a1, unsigned a2, unsigned a3) {
  u64 sv;
  asm volatile(PL("dwordx2", "r0", "m0", "a0") PL("dwordx2", "r1", "m1", "a1") PL("dwordx2", "r2", "m2", "a2") PL("dword", "r3", "r3", "a3") PEND
               : [r0] "+v"(r[0]), [r1] "+v"(r[1]), [r2] "+v"(r[2]), [r3] "+v"(hp), [sv] "=&s"(sv)
               : [m0] "v"(max(r[0][0], r[0][1])), [m1] "v"(max(r[1][0], r[1][1])), [m2] "v"(max(r[2][0], r[2][1])),
                 [a0] "v"(a0), [a1] "v"(a1), [a2] "v"(a2), [a3] "v"(a3), [b] "s"(sb) : "vcc", "memory");
}
__device__ __forceinline__ u32x4 gload16s_w(const void* sbase, unsigned voff) {
  u32x4 r; asm volatile("global_load_dwordx4 %0, %1, %2 sc1\n\ts_waitcnt vmcnt(0)" : "=&v"(r) : "v"(voff), "s"(sbase) : "memory"); return r;
}
__device__ __forceinline__ bf16x8 lds128(lds_cptr p) { return *(const LAS bf16x8*)p; }
__device__ __forceinline__ s16x4 ldstr(lds_cptr p) { return __builtin_amdgcn_ds_read_tr16_b64_v4i16((LAS s16x4*)p); }
__device__ __forceinline__ bf16x8 cat8(s16x4 a, s16x4 b) { return __builtin_shufflevector(a, b, 0, 1, 2, 3, 4, 5, 6, 7); }
__device__ __forceinline__ void st_u64(char* p, u64 v) { __hip_atomic_store((gu64*)p, v, __ATOMIC_RELAXED, __HIP_MEMORY_SCOPE_AGENT); }
__device__ __forceinline__ u64 ld_u64(const char* p) { return __hip_atomic_load((gu64*)p, __ATOMIC_RELAXED, __HIP_MEMORY_SCOPE_AGENT); }
constexpr unsigned SPIN_CAP = 1u << 18;
template <int CTRL> __device__ __forceinline__ unsigned dppu(unsigned x) { return (unsigned)__builtin_amdgcn_mov_dpp((int)x, CTRL, 0xf, 0xf, true); }
template <int CTRL> __device__ __forceinline__ float dppf(float x) { return __uint_as_float(dppu<CTRL>(__float_as_uint(x))); }
__device__ __forceinline__ float sum_x16(float x) { auto s = __builtin_amdgcn_permlane16_swap(__float_as_uint(x), __float_as_uint(x), false, false); return __uint_as_float(s[0]) + __uint_as_float(s[1]); }
__device__ __forceinline__ float sum_x32(float x) { auto s = __builtin_amdgcn_permlane32_swap(__float_as_uint(x), __float_as_uint(x), false, false); return __uint_as_float(s[0]) + __uint_as_float(s[1]); }
__device__ __forceinline__ float sum8(float x) { x += dppf<0xB1>(x); x += dppf<0x4E>(x); x += dppf<0x141>(x); return x; }
__device__ __forceinline__ float sum64(float x) { x = sum8(x); x += dppf<0x140>(x); return sum_x32(sum_x16(x)); }
__device__ __forceinline__ unsigned umax64(unsigned x) {
  x = max(x, dppu<0xB1>(x)); x = max(x, dppu<0x4E>(x)); x = max(x, dppu<0x141>(x)); x = max(x, dppu<0x140>(x));
  auto s = __builtin_amdgcn_permlane16_swap(x, x, false, false); x = max(s[0], s[1]);
  auto t = __builtin_amdgcn_permlane32_swap(x, x, false, false); return max(t[0], t[1]);
}
#define STAMP_DECL
#define STAMP(i)
#define STAMP_FLUSH(kid)

constexpr int P1_APITCH = 1168;
constexpr int P1_WBUF = 27648;
constexpr int P1_WOFF = 64 * P1_APITCH;
constexpr int LDS_BYTES = 163840;
static_assert(P1_WOFF + 3 * P1_WBUF + 128 <= LDS_BYTES, "P1 LDS");
#define WAIT2(lo, A, B) do { if (lo) WAIT_VM(A); else WAIT_VM(B); } while (0)
__device__ __forceinline__ unsigned w_voff(int i, int j) {
  const int R = i / 27, cc = i - R * 27;
  return ((R & ~15) + 4 * (R & 3) + ((R >> 2) & 3)) * 3456 + j * 432 + cc * 16;
}

struct AttnState { f32x16 sA, oA, oB; u32x4 pend; bf16x8 v1p, k1; s16x4 k2; };
#define EXP2(x) __builtin_amdgcn_exp2f(x)
#define G4(P, i, S, r) do { P[i] = cvtpk(EXP2(S[r]), EXP2(S[(r) + 1])); P[(i) + 1] = cvtpk(EXP2(S[(r) + 2]), EXP2(S[(r) + 3])); } while (0)
__device__ __forceinline__ s16x4 ldk2(lds_cptr p) { return __builtin_bit_cast(s16x4, u32x2{*(const LAS unsigned*)p, 0u}); }
__device__ __forceinline__ bf16x8 ldv(lds_cptr p) { return cat8(ldstr(p), ldstr(p + 320)); }

template <class HOOK>
__device__ __forceinline__ void attn8(AttnState& st, lds_cptr kp, lds_cptr k2p, lds_cptr vp, lds_cptr kn, lds_cptr k2n,
                                      bf16x8 qa1, s16x4 qa2, bf16x8 qb1, s16x4 qb2, HOOK hook) {
#pragma unroll
  for (int t = 0; t < 8; ++t) {
    u32x4 p0, p1; f32x16 sB;
    const bf16x8 v0 = ldv(vp + t * 1280), v1 = ldv(vp + t * 1280 + 640);
    const bf16x8 k1n = lds128(t < 7 ? kp + (t + 1) * 1024 : kn);
    const s16x4 k2nx = ldk2(t < 7 ? k2p + (t + 1) * 128 : k2n);
    hook(t);
    SB();
    st.oB = MFMA32(st.v1p, __builtin_bit_cast(bf16x8, st.pend), st.oB); SB();
    G4(p0, 0, st.sA, 0); SB();
    sB = MFMA32(st.k1, qb1, f32x16{}); SB();
    G4(p0, 2, st.sA, 4); SB();
    sB = MFMA32K8(st.k2, qb2, sB); SB();
    G4(p1, 0, st.sA, 8); SB();
    st.oA = MFMA32(v0, __builtin_bit_cast(bf16x8, p0), st.oA); SB();
    G4(p1, 2, st.sA, 12); SB();
    st.oA = MFMA32(v1, __builtin_bit_cast(bf16x8, p1), st.oA); SB();
    G4(p0, 0, sB, 0); SB();
    st.sA = MFMA32(k1n, qa1, f32x16{}); SB();
    G4(p0, 2, sB, 4); SB();
    st.sA = MFMA32K8(k2nx, qa2, st.sA); SB();
    G4(p1, 0, sB, 8); SB();
    st.oB = MFMA32(v0, __builtin_bit_cast(bf16x8, p0), st.oB); SB();
    G4(p1, 2, sB, 12); SB();
    st.pend = p1; st.v1p = v1; st.k1 = k1n; st.k2 = k2nx;
  }
  PIN(st.oA); PIN(st.oB); PIN(st.pend); PIN(st.sA);
}
__device__ __forceinline__ int kv_goff(int u) {
  const int slot = u / 19, pc = u - slot * 19;
  return ((slot & 1) * 4 + (slot >> 1)) * CHUNK_B + pc * 1024;
}
constexpr int P2_XOFF = 82944;
constexpr int P3_XOFF = 116736;
constexpr int FB_OFF = 137216;
constexpr int P3_SOFF = 147456;

__global__ void __launch_bounds__(512, 2) k_block(const char* __restrict__ x, const char* __restrict__ wqkv, const char* __restrict__ wo,
                                                  const char* __restrict__ wr, const char* __restrict__ wg, const char* __restrict__ wu,
                                                  const char* __restrict__ wd, char* __restrict__ out, char* __restrict__ ws) {
  extern __shared__ __attribute__((aligned(16))) char smem[];
  const int tid = threadIdx.x, lane = tid & 63, w = __builtin_amdgcn_readfirstlane(tid >> 6);
  const int b = blockIdx.x, xc = b & 7, sl = b >> 3;
  const unsigned lds0 = (unsigned)(uintptr_t)smem;
  const int h = lane >> 5, r32 = lane & 31;
  STAMP_DECL STAMP(0);
  u64 tag;
  {
    const int j = 4 * (xc >> 2) + (sl >> 3), rb = 8 * (xc & 3) + (sl & 7);
    const int row = tid >> 3, j8 = tid & 7;
    u32x4 xr[9];
    {
      const char* xb = x + (size_t)rb * 64 * 1152;
      const unsigned vo = row * 1152 + j8 * 16;
#pragma unroll
      for (int i = 0; i < 9; ++i) xr[i] = gload16(xb + i * 128, vo);
    }
    u32x2 ep = gload8s(ws, WS_EPOCH);
    const bool lo = w < 3;
    const int rot = sl & 7;
    {
      unsigned voff[4];
#pragma unroll
      for (int p = 0; p < 4; ++p) voff[p] = w_voff(min((w + 8 * p) * 64 + lane, 1727), j);
      const unsigned ldsw = lds0 + P1_WOFF + w * 1024;
#pragma unroll
      for (int c = 0; c < 3; ++c) {
        int kc = c + rot; kc = kc >= 9 ? kc - 9 : kc;
        const char* src = wqkv + (size_t)kc * (64 * 3456);
        const unsigned dst = ldsw + c * P1_WBUF;
        glds16(src, voff[0], dst); glds16(src, voff[1], dst + 8192); glds16(src, voff[2], dst + 16384);
        if (lo) glds16(src, voff[3], dst + 24576);
      }
    }
    WAIT2(lo, 12, 9);
#pragma unroll
    for (int i = 0; i < 9; ++i) PIN(xr[i]);
    PIN(ep);
    tag = (((u64)ep[1] << 32) | ep[0]) * 6364136223846793005ull + 1442695040888963407ull;
    STAMP(1);
    {
      float s = 0.f, q = 0.f;
#pragma unroll
      for (int i = 0; i < 9; ++i)
#pragma unroll
        for (int e = 0; e < 4; ++e) { s = dot2(xr[i][e], 0x3F803F80u, s); q = dot2(xr[i][e], xr[i][e], q); }
      s = sum8(s); q = sum8(q);
      const float m = s * (1.f / 576.f);
      const float var = fmaxf(fmaf(q, 1.f / 576.f, -m * m), 0.f);
      const float rinv = 1.f / rbf(sqrtf(rbf(rbf(var) + 1e-5f)));
      const float nb = -rbf(m) * rinv;
      const lds_ptr ap = (lds_ptr)smem + row * P1_APITCH + j8 * 16;
#pragma unroll
      for (int i = 0; i < 9; ++i) {
        u32x4 o;
#pragma unroll
        for (int e = 0; e < 4; ++e) o[e] = cvtpk(fmaf(bflo(xr[i][e]), rinv, nb), fmaf(bfhi(xr[i][e]), rinv, nb));
        *(LAS u32x4*)(ap + i * 128) = o;
      }
    }
    STAMP(2);
    const u32x4 sent4 = u32x4{SENT, SENT, SENT, SENT};
    if (w == 7) {
      unsigned voff[27];
#pragma unroll
      for (int p = 0; p < 27; ++p) voff[p] = w_voff(p * 64 + lane, j);
#pragma unroll 1
      for (int c = 0; c < 9; ++c) {
        if (c == 0) WAIT_VM(6); else if (c == 1) WAIT_VM(3); else if (c < 8) WAIT_VM(27); else WAIT_VM(0);
        LDS_BAR();
        if (c >= 1 && c <= 6) {
          int kc = c + 2 + rot; kc = kc >= 9 ? kc - 9 : kc;
          const char* src = wqkv + (size_t)kc * (64 * 3456);
          const unsigned dst = lds0 + P1_WOFF + ((c + 2) % 3) * P1_WBUF;
#pragma unroll
          for (int p = 0; p < 27; ++p) glds16(src, voff[p], dst + p * 1024);
        }
      }
      gstore16s(ws, WS_SENT + b * 16384 + tid * 16, sent4);
      gstore16s(ws, WS_SENT + b * 16384 + 8192 + tid * 16, sent4);
    } else {
      const int sg = 4 * ((lane >> 4) & 1) + (lane & 3);
      const int col = w < 6 ? 36 * w + 4 * ((0x86753120u >> (4 * sg)) & 15) : 36 * min(sg, 5) + 16;
      const lds_cptr ap = (lds_cptr)smem + r32 * P1_APITCH + h * 16;
      const lds_cptr bp = (lds_cptr)smem + P1_WOFF + (4 * ((lane & 15) >> 2) + 2 * h) * 432 + col * 2;
      f32x16 acc[2]; acc[0] = f32x16{}; acc[1] = f32x16{};
#pragma unroll
      for (int c = 0; c < 9; ++c) {
        if (c == 0) WAIT2(lo, 8, 6); else if (c == 1) WAIT2(lo, 4, 3); else if (c == 2) WAIT_VM(0);
        LDS_BAR();
        if (c == 2) {
          gstore16s(ws, WS_SENT + b * 16384 + tid * 16, sent4);
          gstore16s(ws, WS_SENT + b * 16384 + 8192 + tid * 16, sent4);
        }
        int kc = c + rot; kc = kc >= 9 ? kc - 9 : kc;
        const lds_cptr a = ap + kc * 128;
#pragma unroll
        for (int ks = 0; ks < 4; ++ks) {
          const bf16x8 wf = cat8(ldstr(bp + (c % 3) * P1_WBUF + ks * 6912), ldstr(bp + (c % 3) * P1_WBUF + ks * 6912 + 432));
          acc[0] = MFMA32(wf, lds128(a + ks * 32), acc[0]);
          acc[1] = MFMA32(wf, lds128(a + 32 * P1_APITCH + ks * 32), acc[1]);
        }
      }
      STAMP(3);
      constexpr float QS = 0.23570226039551584f * 1.4426950408889634f;
      const int t0 = 2 * rb;
      const int kvt = WS_KV + (t0 >> 3) * CHUNK_B;
      if (w < 6) {
        const int P = 6 * j + w, ty = P >> 4, hd = 2 * (P & 15);
        const float sc = ty ? 1.f : QS;
        const int base = ty == 0 ? WS_Q1 + hd * 65536 + t0 * 1024 : kvt + hd * HEAD_B + (ty == 1 ? (t0 & 7) * 1024 : 9216 + (t0 & 7) * 1280);
        const int hs = ty ? HEAD_B : 65536, ts = ty == 2 ? 1280 : 1024;
        const int lo_ = ty == 2 ? (r32 >> 2) * 160 + (r32 & 3) * 32 + h * 16 : lane * 16;
#pragma unroll
        for (int mi = 0; mi < 2; ++mi)
#pragma unroll
          for (int ab = 0; ab < 2; ++ab) {
            u32x4 o;
#pragma unroll
            for (int e = 0; e < 4; ++e) o[e] = cvtpk(acc[mi][8 * ab + 2 * e] * sc, acc[mi][8 * ab + 2 * e + 1] * sc);
            gstore16s(ws, base + ab * hs + mi * ts + lo_, o);
          }
      } else {
#pragma unroll
        for (int rq = 0; rq < 3; ++rq) {
          const int P = 6 * j + 2 * rq + h, ty = P >> 4, hd = 2 * (P & 15);
          const float sc = ty ? 1.f : QS;
#pragma unroll
          for (int mi = 0; mi < 2; ++mi) {
            const unsigned oa = cvtpk(acc[mi][4 * rq] * sc, acc[mi][4 * rq + 1] * sc), ob = cvtpk(acc[mi][4 * rq + 2] * sc, acc[mi][4 * rq + 3] * sc);
            if (ty == 2) {
              const int base = kvt + hd * HEAD_B + 9216 + ((t0 & 7) + mi) * 1280 + (r32 >> 2) * 160 + 128 + (r32 & 3) * 8;
              gstore8s(ws, base, u32x2{oa, 0x00003F80u}); gstore8s(ws, base + HEAD_B, u32x2{ob, 0x00003F80u});
            } else {
              const int base = (ty ? kvt + hd * HEAD_B + 8192 + (t0 & 7) * 128 : WS_Q2 + hd * 8192 + t0 * 128) + mi * 128 + r32 * 4;
              gstore4s(ws, base, oa); gstore4s(ws, base + (ty ? HEAD_B : 8192), ob);
            }
          }
        }
      }
    }
    WAIT_BAR(0);
    if (tid < 8) gstore4s(ws, WS_FLAG1 + tid * 4096 + b * 4, (unsigned)tag);
    STAMP(4);
  }
  const int panel = 4 * xc + (sl & 3), cg = sl >> 2;
  const int mt = w & 3, kq = w >> 2, g = lane >> 4, l16 = lane & 15;
  const int nnt = kq ? 2 : 3, nt0 = kq ? 3 : 0;
  const int tok = panel * 64 + mt * 16 + l16;
  const bool ffn = b < 144;
  const int fs = ((b & 7) + 8 * (b >> 4)) * 2 + ((b >> 3) & 1);
  u32x2 xres[3], wrr[3], gw[9], uw[9];
  unsigned wrow[5];
  u32x4 dw[8];
  {
    const int head = 4 * xc + (sl & 3), qp = sl >> 2;
    const int qpair = w & 3, kh = w >> 2;
    if (w == 0) {
      _Pragma("unroll 1") for (unsigned spin = 0; spin < SPIN_CAP; ++spin) {
        const u32x4 fa = gload16s_w(ws, WS_FLAG1 + (sl & 7) * 4096 + lane * 16);
        const unsigned t0 = (unsigned)tag;
        if (__all(fa[0] == t0 && fa[1] == t0 && fa[2] == t0 && fa[3] == t0)) break;
        __builtin_amdgcn_s_sleep(2);
      }
    }
    LDS_BAR();
    STAMP(5);
    const char* img = ws + WS_KV + (size_t)head * HEAD_B;
    const int qt = qp * 8 + 2 * qpair;
    u32x4 kv[19];
#pragma unroll
    for (int i = 0; i < 5; ++i) kv[i] = gload16s(img + kv_goff(8 * i + w), lane * 16);
    u32x4 q1a = gload16s(ws, WS_Q1 + head * 65536 + qt * 1024 + lane * 16);
    u32x4 q1b = gload16s(ws, WS_Q1 + head * 65536 + qt * 1024 + 1024 + lane * 16);
    unsigned q2a = gload4s(ws, WS_Q2 + head * 8192 + qt * 128 + r32 * 4);
    unsigned q2b = gload4s(ws, WS_Q2 + head * 8192 + qt * 128 + 128 + r32 * 4);
#pragma unroll
    for (int i = 5; i < 8; ++i) kv[i] = gload16s(img + kv_goff(8 * i + w), lane * 16);
    const lds_ptr kdst = (lds_ptr)smem + w * 1024 + lane * 16;
    WAIT_VM(3);
#pragma unroll
    for (int i = 0; i < 5; ++i) { PIN(kv[i]); *(LAS u32x4*)(kdst + i * 8192) = kv[i]; }
    PIN(q1a); PIN(q1b); PIN(q2a); PIN(q2b);
    const bf16x8 qa1 = __builtin_bit_cast(bf16x8, q1a), qb1 = __builtin_bit_cast(bf16x8, q1b);
    const s16x4 qa2 = __builtin_bit_cast(s16x4, u32x2{h ? 0u : q2a, 0u}), qb2 = __builtin_bit_cast(s16x4, u32x2{h ? 0u : q2b, 0u});
    const int vlo = h * 160 + ((lane & 16) ? 128 + (l16 >> 2) * 8 : l16 * 8);
    const lds_cptr kp = (lds_cptr)smem + kh * CHUNK_B + lane * 16;
    const lds_cptr k2p = (lds_cptr)smem + kh * CHUNK_B + 8192 + r32 * 4;
    const lds_cptr vp = (lds_cptr)smem + kh * CHUNK_B + 9216 + vlo;
    LDS_BAR();
    STAMP(6);
    AttnState st;
    st.oA = f32x16{}; st.oB = f32x16{}; st.pend = u32x4{0, 0, 0, 0}; st.v1p = bf16x8{};
    st.k1 = lds128(kp); st.k2 = ldk2(k2p);
    st.sA = MFMA32K8(st.k2, qa2, MFMA32(st.k1, qa1, f32x16{}));
    attn8(st, kp, k2p, vp, kp, k2p, qa1, qa2, qb1, qb2, [&](int t) {
#pragma unroll
      for (int i = 8 + 3 * t; i < 11 + 3 * t; ++i) if (i < 19) kv[i] = gload16s(img + kv_goff(8 * i + w), lane * 16);
    });
    WAIT_VM(0);
#pragma unroll
    for (int i = 5; i < 19; ++i) { PIN(kv[i]); *(LAS u32x4*)(kdst + i * 8192) = kv[i]; }
    LDS_BAR();
    st.k1 = lds128(kp + 2 * CHUNK_B); st.k2 = ldk2(k2p + 2 * CHUNK_B);
    st.sA = MFMA32K8(st.k2, qa2, MFMA32(st.k1, qa1, f32x16{}));
    const unsigned go = ffn ? (tid >> 3) * (F_ * 2) + fs * 64 + (tid & 7) * 8 : 0u;
    attn8(st, kp + 2 * CHUNK_B, k2p + 2 * CHUNK_B, vp + 2 * CHUNK_B, kp + 4 * CHUNK_B, k2p + 4 * CHUNK_B, qa1, qa2, qb1, qb2, [&](int t) {
      gw[t] = gload8(wg + t * 64 * (F_ * 2), go);
      if (t == 7) gw[8] = gload8(wg + 8 * 64 * (F_ * 2), go);
      uw[t] = gload8(wu + t * 64 * (F_ * 2), go);
      if (t == 7) uw[8] = gload8(wu + 8 * 64 * (F_ * 2), go);
    });
    const unsigned dn = ffn ? (fs * 32 + g * 8) * 1152 + w * 144 + min(l16, 8) * 16 : 0u;
    attn8(st, kp + 4 * CHUNK_B, k2p + 4 * CHUNK_B, vp + 4 * CHUNK_B, kp + 6 * CHUNK_B, k2p + 6 * CHUNK_B, qa1, qa2, qb1, qb2, [&](int t) {
      dw[t] = gload16(wd + t * 1152, dn);
      if (t < 3) {
        const int n = min((nt0 + t) * 16 + 4 * g, 68);
        xres[t] = gload8(x, tok * 1152 + (cg * 72 + n) * 2);
        wrr[t] = gload8(wr, (cg * 72 + n) * 2);
      } else {
        wrow[t - 3] = gload4(wr, min((t - 3) * 64 + lane, 287) * 4);
      }
    });
    LDS_BAR();
    attn8(st, kp + 6 * CHUNK_B, k2p + 6 * CHUNK_B, vp + 6 * CHUNK_B, kp + 6 * CHUNK_B, k2p + 6 * CHUNK_B, qa1, qa2, qb1, qb2, [&](int t) {
#pragma unroll
      for (int i = 3 * t; i < 3 * t + 3; ++i) {
        if (i < 11) {
          const int pc = i < 10 ? 8 * i + w : 80;
          const int ci = pc * 64 + lane;
          const int R = ci / 9, cc = ci - R * 9;
          glds16(wo, R * 1152 + cg * 144 + cc * 16, lds0 + pc * 1024);
        }
      }
    });
    st.oB = MFMA32(st.v1p, __builtin_bit_cast(bf16x8, st.pend), st.oB);
    STAMP(7);
    LAS f32x4* xw = (LAS f32x4*)(smem + P2_XOFF) + (qpair * 2 + kh) * 192 + lane;
    LAS f32x4* xr_ = (LAS f32x4*)(smem + P2_XOFF) + (qpair * 2 + (kh ^ 1)) * 192 + lane;
    f32x16 o = kh ? st.oB : st.oA;
    {
      const f32x16 give = kh ? st.oA : st.oB;
#pragma unroll
      for (int r = 0; r < 3; ++r) xw[r * 64] = f32x4{give[4 * r], give[4 * r + 1], give[4 * r + 2], give[4 * r + 3]};
    }
    WAIT_BAR(0);
#pragma unroll
    for (int r = 0; r < 3; ++r) {
      const f32x4 t = xr_[r * 64];
#pragma unroll
      for (int e = 0; e < 4; ++e) o[4 * r + e] += t[e];
    }
    float l;
    {
      auto sw = __builtin_amdgcn_permlane32_swap(__float_as_uint(o[10]), __float_as_uint(o[10]), false, false);
      l = __uint_as_float(sw[0]);
    }
    bool ok = (l > 8.6736174e-19f) && (l < 1.1529215e18f);
#pragma unroll
    for (int r = 0; r < 10; ++r) ok = ok && ((h && r >= 8) || fabsf(o[r]) < 3.0e38f);
    if (!__all(ok)) {
      const bf16x8 qm1 = kh ? qb1 : qa1; const s16x4 qm2 = kh ? qb2 : qa2;
      const lds_ptr fb = (lds_ptr)smem + FB_OFF + w * 1280;
      float mx = 0.f;
#pragma unroll 1
      for (int pass = 0; pass < 2; ++pass) {
        f32x16 cin;
#pragma unroll
        for (int r = 0; r < 16; ++r) cin[r] = -mx;
        float m2 = -INFINITY;
        o = f32x16{};
#pragma unroll 1
        for (int kt = 0; kt < 64; ++kt) {
          const char* cb = img + (kt >> 3) * CHUNK_B;
          u32x4 k1 = gload16s(cb + (kt & 7) * 1024, lane * 16);
          unsigned k2 = gload4s(cb + 8192 + (kt & 7) * 128, r32 * 4);
          u32x4 va = gload16s(cb + 9216 + (kt & 7) * 1280, lane * 16), vb = gload16s(cb + 9216 + (kt & 7) * 1280 + 1024, l16 * 16);
          WAIT_VM(0); PIN(k1); PIN(k2); PIN(va); PIN(vb);
          *(LAS u32x4*)(fb + lane * 16) = va; *(LAS u32x4*)(fb + 1024 + l16 * 16) = vb;
          f32x16 s = MFMA32(__builtin_bit_cast(bf16x8, k1), qm1, cin);
          s = MFMA32K8(__builtin_bit_cast(s16x4, (u32x2{k2, 0u})), qm2, s);
#pragma unroll
          for (int r = 0; r < 16; ++r) m2 = fmaxf(m2, s[r]);
          u32x4 p0, p1;
          G4(p0, 0, s, 0); G4(p0, 2, s, 4); G4(p1, 0, s, 8); G4(p1, 2, s, 12);
          asm volatile("s_waitcnt lgkmcnt(0)" ::: "memory");
          o = MFMA32(ldv((lds_cptr)fb + vlo), __builtin_bit_cast(bf16x8, p0), o);
          o = MFMA32(ldv((lds_cptr)fb + vlo + 640), __builtin_bit_cast(bf16x8, p1), o);
          asm volatile("s_waitcnt lgkmcnt(0)" ::: "memory");
        }
        auto sw = __builtin_amdgcn_permlane32_swap(__float_as_uint(m2), __float_as_uint(m2), false, false);
        mx = fmaxf(__uint_as_float(sw[0]), __uint_as_float(sw[1]));
      }
      auto sw = __builtin_amdgcn_permlane32_swap(__float_as_uint(o[10]), __float_as_uint(o[10]), false, false);
      l = __uint_as_float(sw[0]);
    }
    const float rl = 1.f / l;
    u32x4 ov;
#pragma unroll
    for (int e = 0; e < 4; ++e) ov[e] = nsent(cvtpk(o[2 * e] * rl, o[2 * e + 1] * rl));
    gstore16s(ws, WS_O1 + head * 65536 + (qt + kh) * 1024 + lane * 16, ov);
    if (!h) gstore4s(ws, WS_O2 + ((qt + kh) * 32 + r32) * 128 + head * 4, nsent(cvtpk(o[8] * rl, o[9] * rl)));
    STAMP(8);
  }
  float wsum;
  {
#pragma unroll
    for (int i = 0; i < 5; ++i) PIN(wrow[i]);
    if (h) wrow[4] = 0u;
    float t = 0.f;
#pragma unroll
    for (int i = 0; i < 5; ++i) t = dot2(wrow[i], 0x3F803F80u, t);
    wsum = sum64(t);
  }
  f32x4 acc[5];
  {
    u32x4 of[9];
    const unsigned o1 = WS_O1 + (kq * 18 + (g >> 1)) * 65536 + (2 * panel + (mt >> 1)) * 1024 + (g & 1) * 512 + ((mt & 1) * 16 + l16) * 16;
    const unsigned o2 = WS_O2 + tok * 128 + g * 16;
    const unsigned a7 = kq ? o2 : o1 + 7 * 131072, a8 = kq ? o2 + 64 : o1 + 8 * 131072;
#pragma unroll
    for (int i = 0; i < 9; ++i) of[i] = u32x4{SENT, SENT, SENT, SENT};
    _Pragma("unroll 1") for (unsigned spin = 0; spin < SPIN_CAP; ++spin) {
      poll_o(of, ws, o1, a7, a8);
      unsigned m = 0;
#pragma unroll
      for (int i = 0; i < 9; ++i) m = max(m, mx4(of[i]));
      if (__all(m != SENT)) break;
      __builtin_amdgcn_s_sleep(2);
    }
    STAMP(9);
    const int q = l16 >> 2;
    const lds_cptr bp = (lds_cptr)smem + (kq * 9 * 36 + 20 * (g >> 1) + 4 * (g & 1) + q) * 144 + (lane & 3) * 8;
    const lds_cptr bp2 = (lds_cptr)smem + (18 * (4 * g + (q >> 1)) + ((q >> 1) ? 0 : 16) + (q & 1)) * 144 + (lane & 3) * 8;
#pragma unroll
    for (int i = 0; i < 5; ++i) acc[i] = f32x4{};
#pragma unroll
    for (int ks = 0; ks < 9; ++ks) {
      const bool sp = ks >= 7 && kq;
      const lds_cptr b0 = ks >= 7 ? (sp ? bp2 + (ks - 7) * (288 * 144) : bp + ks * (36 * 144)) : bp + ks * (36 * 144);
      const int d2 = sp ? 36 * 144 : 8 * 144;
#pragma unroll
      for (int i = 0; i < 5; ++i) {
        const bf16x8 wf = cat8(ldstr(b0 + i * 32), ldstr(b0 + d2 + i * 32));
        acc[i] = MFMA16(wf, __builtin_bit_cast(bf16x8, of[ks]), acc[i]);
      }
    }
    LAS f32x4* xw = (LAS f32x4*)(smem + P3_XOFF) + mt * 320 + lane;
    if (kq == 0) { xw[3 * 64] = acc[3]; xw[4 * 64] = acc[4]; } else { xw[0] = acc[0]; xw[64] = acc[1]; xw[128] = acc[2]; }
    LDS_BAR();
    if (kq == 0) { acc[0] += xw[0]; acc[1] += xw[64]; acc[2] += xw[128]; }
    else { acc[0] = acc[3] + xw[3 * 64]; acc[1] = acc[4] + xw[4 * 64]; acc[2] = f32x4{}; }
  }
#pragma unroll
  for (int i = 0; i < 3; ++i) { PIN(xres[i]); PIN(wrr[i]); }
  STAMP(10);
  const int rgp = panel * 4 + mt;
  float hv[3][4];
  unsigned key; float mb, rinv;
  {
    float s1 = 0.f, s2 = 0.f, s3 = 0.f;
#pragma unroll
    for (int i = 0; i < 3; ++i) {
      const bool valid = (i < nnt) && ((nt0 + i) * 16 + 4 * g < 72);
      const unsigned a01 = cvtpk(acc[i][0], acc[i][1]), a23 = cvtpk(acc[i][2], acc[i][3]);
      unsigned h01 = cvtpk(bflo(xres[i][0]) + bflo(a01), bfhi(xres[i][0]) + bfhi(a01));
      unsigned h23 = cvtpk(bflo(xres[i][1]) + bflo(a23), bfhi(xres[i][1]) + bfhi(a23));
      h01 = valid ? h01 : 0u; h23 = valid ? h23 : 0u;
      hv[i][0] = bflo(h01); hv[i][1] = bfhi(h01); hv[i][2] = bflo(h23); hv[i][3] = bfhi(h23);
      s1 += (hv[i][0] + hv[i][1]) + (hv[i][2] + hv[i][3]);
      s2 = fmaf(hv[i][0], hv[i][0], fmaf(hv[i][1], hv[i][1], fmaf(hv[i][2], hv[i][2], fmaf(hv[i][3], hv[i][3], s2))));
      s3 = fmaf(hv[i][0], bflo(wrr[i][0]), fmaf(hv[i][1], bfhi(wrr[i][0]), fmaf(hv[i][2], bflo(wrr[i][1]), fmaf(hv[i][3], bfhi(wrr[i][1]), s3))));
    }
    s1 = sum_x32(sum_x16(s1)); s2 = sum_x32(sum_x16(s2)); s3 = sum_x32(sum_x16(s3));
    if (g == 0) gstore16s(ws, SY_STATS + rgp * 4096 + (cg * 2 + kq) * 256 + l16 * 16,
                          u32x4{nsent(__float_as_uint(s1)), nsent(__float_as_uint(s2)), nsent(__float_as_uint(s3)), 0u});
    u32x4 sv[4];
#pragma unroll
    for (int i = 0; i < 4; ++i) sv[i] = u32x4{SENT, SENT, SENT, SENT};
    _Pragma("unroll 1") for (unsigned spin = 0; spin < SPIN_CAP; ++spin) {
      poll_4x16(sv, ws, SY_STATS + rgp * 4096 + lane * 16);
      if (__all(max(max(mx4(sv[0]), mx4(sv[1])), max(mx4(sv[2]), mx4(sv[3]))) != SENT)) break;
      __builtin_amdgcn_s_sleep(1);
    }
    STAMP(11);
#define SUMC(c) sum_x32(sum_x16((__uint_as_float(sv[0][c]) + __uint_as_float(sv[1][c])) + (__uint_as_float(sv[2][c]) + __uint_as_float(sv[3][c]))))
    s1 = SUMC(0); s2 = SUMC(1); s3 = SUMC(2);
    const float m = s1 * (1.f / 576.f);
    mb = rbf(m);
    rinv = 1.f / rbf(sqrtf(rbf(rbf(fmaxf(fmaf(s2, 1.f / 576.f, -m * m), 0.f)) + 1e-5f)));
    const unsigned u = __float_as_uint((s3 - mb * wsum) * rinv);
    const unsigned ord = (u & 0x80000000u) ? ~u : (u | 0x80000000u);
    key = (min(ord, 0xFFFFF000u) & ~0x7FFu) | (unsigned)(2047 - tok);
    key = max(key, dppu<0xB1>(key)); key = max(key, dppu<0x4E>(key)); key = max(key, dppu<0x141>(key)); key = max(key, dppu<0x140>(key));
  }
  if (cg == 0 && kq == 0 && lane < 16) gstore4s(ws, SY_BEST + lane * 4096 + rgp * 4, key);
  const bool isbest = tok == 2047 - (int)(key & 0x7FFu);
  u32x2 ov[3];
#pragma unroll
  for (int i = 0; i < 3; ++i) {
    const bool valid = (i < nnt) && ((nt0 + i) * 16 + 4 * g < 72);
    const unsigned d01 = cvtpk(hv[i][0] - mb, hv[i][1] - mb), d23 = cvtpk(hv[i][2] - mb, hv[i][3] - mb);
    const unsigned n01 = cvtpk(bflo(d01) * rinv, bfhi(d01) * rinv), n23 = cvtpk(bflo(d23) * rinv, bfhi(d23) * rinv);
    ov[i] = u32x2{cvtpk(hv[i][0] + bflo(n01), hv[i][1] + bfhi(n01)), cvtpk(hv[i][2] + bflo(n23), hv[i][3] + bfhi(n23))};
    const int col = cg * 72 + (nt0 + i) * 16 + 4 * g;
    if (valid && isbest) {
      gstore8s(ws, SY_CHN + rgp * 1152 + col * 2, u32x2{nsent(n01), nsent(n23)});
      gstore8s(ws, SY_CH + rgp * 1152 + col * 2, u32x2{nsent(cvtpk(hv[i][0], hv[i][1])), nsent(cvtpk(hv[i][2], hv[i][3]))});
    }
  }
#pragma unroll
  for (int i = 0; i < 3; ++i) {
    const bool valid = (i < nnt) && ((nt0 + i) * 16 + 4 * g < 72);
    if (valid && !isbest) *(u32x2*)(out + (size_t)tok * 1152 + (cg * 72 + (nt0 + i) * 16 + 4 * g) * 2) = ov[i];
  }
  LAS unsigned* xs = (LAS unsigned*)(smem + P3_SOFF + 8192);
  if (w == 0) {
    u32x2 a = u32x2{SENT, SENT};
    _Pragma("unroll 1") for (unsigned spin = 0; spin < SPIN_CAP; ++spin) {
      poll_8(a, ws, SY_BEST + (b & 15) * 4096 + lane * 8);
      if (__all(max(a[0], a[1]) != SENT)) break;
      __builtin_amdgcn_s_sleep(1);
    }
    const int s0 = __builtin_amdgcn_readfirstlane(2047 - (int)(umax64(max(a[0], a[1])) & 0x7FFu));
    STAMP(12);
    if (b < 240) {
      unsigned xv[5] = {SENT, SENT, SENT, SENT, SENT};
      _Pragma("unroll 1") for (unsigned spin = 0; spin < SPIN_CAP; ++spin) {
        poll_5x4(xv, ws, SY_CHN + (s0 >> 4) * 1152 + lane * 4, SY_CHN + (s0 >> 4) * 1152 + 1024 + r32 * 4);
        unsigned m = 0;
#pragma unroll
        for (int i = 0; i < 5; ++i) m = max(m, xv[i]);
        if (__all(m != SENT)) break;
      }
#pragma unroll
      for (int i = 0; i < 5; ++i) xs[i * 64 + lane] = xv[i];
    }
    xs[320] = s0;
  }
  LDS_BAR();
  const int sel = __builtin_amdgcn_readfirstlane(xs[320]);
  const int wgrp = sel >> 4;
  if (ffn) {
#pragma unroll
    for (int i = 0; i < 9; ++i) { PIN(gw[i]); PIN(uw[i]); }
#pragma unroll
    for (int i = 0; i < 8; ++i) PIN(dw[i]);
    STAMP(13);
    float ga[4] = {0.f, 0.f, 0.f, 0.f}, ua[4] = {0.f, 0.f, 0.f, 0.f};
#pragma unroll
    for (int i = 0; i < 9; ++i) {
      const float xf = bflo(*((LAS unsigned short*)xs + (tid >> 3) + 64 * i));
      ga[0] = fmaf(xf, bflo(gw[i][0]), ga[0]); ga[1] = fmaf(xf, bfhi(gw[i][0]), ga[1]); ga[2] = fmaf(xf, bflo(gw[i][1]), ga[2]); ga[3] = fmaf(xf, bfhi(gw[i][1]), ga[3]);
      ua[0] = fmaf(xf, bflo(uw[i][0]), ua[0]); ua[1] = fmaf(xf, bfhi(uw[i][0]), ua[1]); ua[2] = fmaf(xf, bflo(uw[i][1]), ua[2]); ua[3] = fmaf(xf, bfhi(uw[i][1]), ua[3]);
    }
    LAS float* gp = (LAS float*)(smem + P3_SOFF);
#pragma unroll
    for (int e = 0; e < 4; ++e) { ga[e] = sum_x32(sum_x16(ga[e])); ua[e] = sum_x32(sum_x16(ua[e])); }
    if (lane < 16) {
      *(LAS f32x4*)(gp + (w * 2 + (lane >> 3)) * 64 + (lane & 7) * 4) = f32x4{ga[0], ga[1], ga[2], ga[3]};
      *(LAS f32x4*)(gp + (w * 2 + (lane >> 3)) * 64 + 32 + (lane & 7) * 4) = f32x4{ua[0], ua[1], ua[2], ua[3]};
    }
    LDS_BAR();
    float av;
    {
      float s = 0.f;
#pragma unroll
      for (int i = 0; i < 16; ++i) s += gp[i * 64 + lane];
      s = rbf(s);
      auto sw = __builtin_amdgcn_permlane32_swap(__float_as_uint(s), __float_as_uint(s), false, false);
      const float gs = __uint_as_float(sw[0]), us = __uint_as_float(sw[1]);
      av = rbf(rbf(gs / (1.f + __expf(-gs))) * us);
    }
    float y[8] = {0.f, 0.f, 0.f, 0.f, 0.f, 0.f, 0.f, 0.f};
#pragma unroll
    for (int i = 0; i < 8; ++i) {
      const float a = __shfl(av, g * 8 + i);
#pragma unroll
      for (int e = 0; e < 4; ++e) { y[2 * e] = fmaf(a, bflo(dw[i][e]), y[2 * e]); y[2 * e + 1] = fmaf(a, bfhi(dw[i][e]), y[2 * e + 1]); }
    }
#pragma unroll
    for (int e = 0; e < 8; ++e) y[e] = sum_x32(sum_x16(y[e]));
    const float y0 = g == 0 ? y[0] : g == 1 ? y[2] : g == 2 ? y[4] : y[6], y1 = g == 0 ? y[1] : g == 1 ? y[3] : g == 2 ? y[5] : y[7];
    if (l16 < 9) gstore8s(ws, SY_PART + fs * 2304 + (w * 72 + l16 * 8 + g * 2) * 4, u32x2{nsent(__float_as_uint(y0)), nsent(__float_as_uint(y1))});
  }
  if (wgrp != rgp && isbest) {
#pragma unroll
    for (int i = 0; i < 3; ++i) {
      const bool valid = (i < nnt) && ((nt0 + i) * 16 + 4 * g < 72);
      if (valid) *(u32x2*)(out + (size_t)tok * 1152 + (cg * 72 + (nt0 + i) * 16 + 4 * g) * 2) = ov[i];
    }
  }
  if (!ffn && b < 240 && w < 3) {
    const int cp = (b - 144) * 3 + w;
    float r = 0.f;
#pragma unroll
    for (int i = 0; i < 5; ++i) { const unsigned xv = xs[i * 64 + lane]; r = fmaf(bflo(xv), bflo(wrow[i]), fmaf(bfhi(xv), bfhi(wrow[i]), r)); }
    const unsigned np = xs[cp];
    r = rbf(sum64(r));
    const float sig = rbf(1.f / (1.f + __expf(-r)));
    STAMP(13);
    u32x2 pv[3] = {u32x2{SENT, SENT}, u32x2{SENT, SENT}, u32x2{SENT, SENT}};
    unsigned hp = SENT;
    _Pragma("unroll 1") for (unsigned spin = 0; spin < SPIN_CAP; ++spin) {
      poll_part(pv, hp, ws, SY_PART + lane * 2304 + cp * 8, SY_PART + (64 + lane) * 2304 + cp * 8, SY_PART + (128 + l16) * 2304 + cp * 8,
                SY_CH + wgrp * 1152 + cp * 4);
      unsigned m = hp;
#pragma unroll
      for (int i = 0; i < 3; ++i) m = max(m, max(pv[i][0], pv[i][1]));
      if (__all(m != SENT)) break;
      __builtin_amdgcn_s_sleep(1);
    }
    const bool v2 = lane < 16;
    const float y0 = sum64(__uint_as_float(pv[0][0]) + __uint_as_float(pv[1][0]) + (v2 ? __uint_as_float(pv[2][0]) : 0.f));
    const float y1 = sum64(__uint_as_float(pv[0][1]) + __uint_as_float(pv[1][1]) + (v2 ? __uint_as_float(pv[2][1]) : 0.f));
    const float r0 = bflo(hp) + rbf(bflo(np) + rbf(rbf(y0) * sig));
    const float r1 = bfhi(hp) + rbf(bfhi(np) + rbf(rbf(y1) * sig));
    if (lane == 0) *(unsigned*)(out + (size_t)sel * 1152 + cp * 4) = cvtpk(r0, r1);
    if (b == 144 && tid == 0) st_u64(ws + WS_EPOCH, tag);
  }
  STAMP_FLUSH(0);
}

extern "C" void kernel_launch(void* const* d_in, const int* in_sizes, int n_in, void* d_out, int out_size, void* d_ws,
                              size_t ws_size, hipStream_t stream) {
  (void)hipFuncSetAttribute((const void*)k_block, hipFuncAttributeMaxDynamicSharedMemorySize, LDS_BYTES);
  k_block<<<256, 512, LDS_BYTES, stream>>>((const char*)d_in[0], (const char*)d_in[1], (const char*)d_in[2], (const char*)d_in[4],
                                           (const char*)d_in[5], (const char*)d_in[6], (const char*)d_in[7], (char*)d_out, (char*)d_ws);
}
```

```cpp
#include <hip/hip_runtime.h>
#include <hip/hip_bf16.h>

typedef __attribute__((ext_vector_type(8))) short bf16x8;
typedef __attribute__((ext_vector_type(4))) short s16x4;
typedef __attribute__((ext_vector_type(16))) float f32x16;
typedef __attribute__((ext_vector_type(4))) float f32x4;
typedef __attribute__((ext_vector_type(4))) unsigned u32x4;
typedef __attribute__((ext_vector_type(2))) unsigned u32x2;
typedef __attribute__((ext_vector_type(2))) __bf16 b2_t;
typedef unsigned long long u64;
#define LAS __attribute__((address_space(3)))
#define GAS __attribute__((address_space(1)))
typedef LAS const char* lds_cptr;
typedef LAS char* lds_ptr;
typedef GAS u64 gu64;
typedef GAS unsigned gu32;

constexpr int S_ = 2048, D_ = 576, H_ = 32, F_ = 4608;
constexpr int CHUNK_B = 19456;
constexpr int HEAD_B = 8 * CHUNK_B;
constexpr int WS_EPOCH = 0;
constexpr int WS_FLAG1 = 4096;
constexpr int WS_KV = 8192;
constexpr int WS_Q1 = WS_KV + H_ * HEAD_B;
constexpr int WS_Q2 = WS_Q1 + H_ * 65536;
constexpr int WS_SENT = WS_Q2 + H_ * 8192;
constexpr int WS_O1 = WS_SENT;
constexpr int WS_O2 = WS_O1 + H_ * 65536;
constexpr int SY_STATS = WS_O2 + S_ * 128;
constexpr int SY_BEST = SY_STATS + 128 * 4096;
constexpr int SY_CHN = SY_BEST + 16 * 4096;
constexpr int SY_CH = SY_CHN + 128 * 1152;
constexpr int SY_PART = SY_CH + 128 * 1152;
constexpr int SY_END = SY_PART + 144 * 2304;
static_assert(SY_END - WS_SENT <= 256 * 16384, "sentinel area");
constexpr unsigned SENT = 0xFFFFFFFFu;

#define MFMA32(a, b, c) __builtin_amdgcn_mfma_f32_32x32x16_bf16(a, b, c, 0, 0, 0)
#define MFMA32K8(a, b, c) __builtin_amdgcn_mfma_f32_32x32x8bf16_1k(a, b, c, 0, 0, 0)
#define MFMA16(a, b, c) __builtin_amdgcn_mfma_f32_16x16x32_bf16(a, b, c, 0, 0, 0)
#define WAIT_VM(N) asm volatile("s_waitcnt vmcnt(" #N ")" ::: "memory")
#define WAIT_BAR(N) asm volatile("s_waitcnt vmcnt(" #N ") lgkmcnt(0)\n\ts_barrier" ::: "memory")
#define LDS_BAR() asm volatile("s_waitcnt lgkmcnt(0)\n\ts_barrier" ::: "memory")
#define PIN(x) asm volatile("" : "+v"(x))
#define SB() __builtin_amdgcn_sched_barrier(0)

__device__ __forceinline__ unsigned cvtpk(float lo, float hi) {
  typedef __attribute__((ext_vector_type(2))) float f2_t;
  return __builtin_bit_cast(unsigned, __builtin_convertvector((f2_t{lo, hi}), b2_t));
}
__device__ __forceinline__ float bflo(unsigned u) { return __uint_as_float(u << 16); }
__device__ __forceinline__ float bfhi(unsigned u) { return __uint_as_float(u & 0xffff0000u); }
__device__ __forceinline__ float rbf(float x) { return bflo(cvtpk(x, x)); }
__device__ __forceinline__ float dot2(unsigned a, unsigned b, float c) {
  return __builtin_amdgcn_fdot2_f32_bf16(__builtin_bit_cast(b2_t, a), __builtin_bit_cast(b2_t, b), c, false);
}
__device__ __forceinline__ void glds16(const void* sbase, unsigned voff, unsigned lds_dst) {
  unsigned sv;
  asm volatile("s_mov_b32 %0, m0\n\ts_mov_b32 m0, %3\n\ts_nop 0\n\tglobal_load_lds_dwordx4 %1, %2\n\ts_mov_b32 m0, %0"
               : "=&s"(sv) : "v"(voff), "s"(sbase), "s"(lds_dst) : "memory");
}
__device__ __forceinline__ u32x4 gload16(const void* sbase, unsigned voff) {
  u32x4 r; asm volatile("global_load_dwordx4 %0, %1, %2" : "=v"(r) : "v"(voff), "s"(sbase) : "memory"); return r;
}
__device__ __forceinline__ u32x2 gload8(const void* sbase, unsigned voff) {
  u32x2 r; asm volatile("global_load_dwordx2 %0, %1, %2" : "=v"(r) : "v"(voff), "s"(sbase) : "memory"); return r;
}
__device__ __forceinline__ u32x4 gload16s(const void* sbase, unsigned voff) {
  u32x4 r; asm volatile("global_load_dwordx4 %0, %1, %2 sc1" : "=v"(r) : "v"(voff), "s"(sbase) : "memory"); return r;
}
__device__ __forceinline__ u32x2 gload8s(const void* sbase, unsigned voff) {
  u32x2 r; asm volatile("global_load_dwordx2 %0, %1, %2 sc1" : "=v"(r) : "v"(voff), "s"(sbase) : "memory"); return r;
}
__device__ __forceinline__ unsigned gload4s(const void* sbase, unsigned voff) {
  unsigned r; asm volatile("global_load_dword %0, %1, %2 sc1" : "=v"(r) : "v"(voff), "s"(sbase) : "memory"); return r;
}
__device__ __forceinline__ void gstore16s(void* sbase, unsigned voff, u32x4 v) {
  asm volatile("global_store_dwordx4 %0, %1, %2 sc1\n\ts_nop 1" :: "v"(voff), "v"(v), "s"(sbase) : "memory");
}
__device__ __forceinline__ void gstore4s(void* sbase, unsigned voff, unsigned v) {
  asm volatile("global_store_dword %0, %1, %2 sc1" :: "v"(voff), "v"(v), "s"(sbase) : "memory");
}
__device__ __forceinline__ void gstore8s(void* sbase, unsigned voff, u32x2 v) {
  asm volatile("global_store_dwordx2 %0, %1, %2 sc1" :: "v"(voff), "v"(v), "s"(sbase) : "memory");
}
__device__ __forceinline__ unsigned gload4(const void* sbase, unsigned voff) {
  unsigned r; asm volatile("global_load_dword %0, %1, %2" : "=v"(r) : "v"(voff), "s"(sbase) : "memory"); return r;
}
__device__ __forceinline__ unsigned nsent(unsigned v) { return min(v, SENT - 1u); }
__device__ __forceinline__ unsigned mx4(u32x4 v) { return max(max(v[0], v[1]), max(v[2], v[3])); }
#define PL(W, R, M, A) "v_cmp_eq_u32 vcc, -1, %[" M "]\n\ts_and_saveexec_b64 %[sv], vcc\n\tglobal_load_" W " %[" R "], %[" A "], %[b] sc1\n\ts_mov_b64 exec, %[sv]\n\t"
#define PADD(N) "v_add_u32 %[t], " N ", %[t]\n\t"
#define PEND "s_waitcnt vmcnt(0)"
#define PL7 PL("dwordx4", "r0", "m0", "t") PADD("0x20000") PL("dwordx4", "r1", "m1", "t") PADD("0x20000") PL("dwordx4", "r2", "m2", "t") PADD("0x20000") \
            PL("dwordx4", "r3", "m3", "t") PADD("0x20000") PL("dwordx4", "r4", "m4", "t") PADD("0x20000") PL("dwordx4", "r5", "m5", "t") PADD("0x20000") \
            PL("dwordx4", "r6", "m6", "t")
#define PL7O [r0] "+v"(r[0]), [r1] "+v"(r[1]), [r2] "+v"(r[2]), [r3] "+v"(r[3]), [r4] "+v"(r[4]), [r5] "+v"(r[5]), [r6] "+v"(r[6]), [sv] "=&s"(sv), [t] "+v"(a0)
#define PL7I [m0] "v"(mx4(r[0])), [m1] "v"(mx4(r[1])), [m2] "v"(mx4(r[2])), [m3] "v"(mx4(r[3])), [m4] "v"(mx4(r[4])), [m5] "v"(mx4(r[5])), \
             [m6] "v"(mx4(r[6])), [b] "s"(sb)
__device__ __forceinline__ void poll_o9(u32x4 (&r)[9], const void* sb, unsigned a0) {
  u64 sv;
  asm volatile(PL7 PADD("0x20000") PL("dwordx4", "r7", "m7", "t") PADD("0x20000") PL("dwordx4", "r8", "m8", "t") PEND
               : PL7O, [r7] "+v"(r[7]), [r8] "+v"(r[8]) : PL7I, [m7] "v"(mx4(r[7])), [m8] "v"(mx4(r[8])) : "vcc", "memory");
}
#define PLD(R) PL("dword", R, R, "u")
__device__ __forceinline__ void poll_o7(u32x4 (&r)[9], unsigned (&d)[8], const void* sb, unsigned a0, unsigned a2) {
  u64 sv;
  asm volatile(PL7 PLD("d0") "v_add_u32 %[u], 0x2000, %[u]\n\t" PLD("d1") "v_add_u32 %[u], 0x2000, %[u]\n\t" PLD("d2") "v_add_u32 %[u], 0x2000, %[u]\n\t"
               PLD("d3") "v_add_u32 %[u], 0x1a000, %[u]\n\t" PLD("d4") "v_add_u32 %[u], 0x2000, %[u]\n\t" PLD("d5") "v_add_u32 %[u], 0x2000, %[u]\n\t"
               PLD("d6") "v_add_u32 %[u], 0x2000, %[u]\n\t" PLD("d7") PEND
               : PL7O, [u] "+v"(a2), [d0] "+v"(d[0]), [d1] "+v"(d[1]), [d2] "+v"(d[2]), [d3] "+v"(d[3]), [d4] "+v"(d[4]), [d5] "+v"(d[5]),
                 [d6] "+v"(d[6]), [d7] "+v"(d[7]) : PL7I : "vcc", "memory");
}
__device__ __forceinline__ void poll_4x16(u32x4 (&r)[4], const void* sb, unsigned a0) {
  u64 sv;
  asm volatile(PL("dwordx4", "r0", "m0", "t") PADD("0x400") PL("dwordx4", "r1", "m1", "t") PADD("0x400") PL("dwordx4", "r2", "m2", "t") PADD("0x400")
               PL("dwordx4", "r3", "m3", "t") PEND
               : [r0] "+v"(r[0]), [r1] "+v"(r[1]), [r2] "+v"(r[2]), [r3] "+v"(r[3]), [sv] "=&s"(sv), [t] "+v"(a0)
               : [m0] "v"(mx4(r[0])), [m1] "v"(mx4(r[1])), [m2] "v"(mx4(r[2])), [m3] "v"(mx4(r[3])), [b] "s"(sb) : "vcc", "memory");
}
__device__ __forceinline__ void poll_8(u32x2& r, const void* sb, unsigned a0) {
  u64 sv;
  asm volatile(PL("dwordx2", "r0", "m0", "a0") PEND
               : [r0] "+v"(r), [sv] "=&s"(sv) : [m0] "v"(max(r[0], r[1])), [a0] "v"(a0), [b] "s"(sb) : "vcc", "memory");
}
__device__ __forceinline__ void poll_5x4(unsigned (&r)[5], const void* sb, unsigned a0, unsigned a4) {
  u64 sv;
  asm volatile(PL("dword", "r0", "r0", "t") PADD("0x100") PL("dword", "r1", "r1", "t") PADD("0x100") PL("dword", "r2", "r2", "t") PADD("0x100")
               PL("dword", "r3", "r3", "t") PL("dword", "r4", "r4", "a4") PEND
               : [r0] "+v"(r[0]), [r1] "+v"(r[1]), [r2] "+v"(r[2]), [r3] "+v"(r[3]), [r4] "+v"(r[4]), [sv] "=&s"(sv), [t] "+v"(a0)
               : [a4] "v"(a4), [b] "s"(sb) : "vcc", "memory");
}
__device__ __forceinline__ void poll_part(u32x2 (&r)[3], unsigned& hp, const void* sb, unsigned a0, unsigned a1, unsigned a2, unsigned a3) {
  u64 sv;
  asm volatile(PL("dwordx2", "r0", "m0", "a0") PL("dwordx2", "r1", "m1", "a1") PL("dwordx2", "r2", "m2", "a2") PL("dword", "r3", "r3", "a3") PEND
               : [r0] "+v"(r[0]), [r1] "+v"(r[1]), [r2] "+v"(r[2]), [r3] "+v"(hp), [sv] "=&s"(sv)
               : [m0] "v"(max(r[0][0], r[0][1])), [m1] "v"(max(r[1][0], r[1][1])), [m2] "v"(max(r[2][0], r[2][1])),
                 [a0] "v"(a0), [a1] "v"(a1), [a2] "v"(a2), [a3] "v"(a3), [b] "s"(sb) : "vcc", "memory");
}
__device__ __forceinline__ u32x4 gload16s_w(const void* sbase, unsigned voff) {
  u32x4 r; asm volatile("global_load_dwordx4 %0, %1, %2 sc1\n\ts_waitcnt vmcnt(0)" : "=&v"(r) : "v"(voff), "s"(sbase) : "memory"); return r;
}
__device__ __forceinline__ bf16x8 lds128(lds_cptr p) { return *(const LAS bf16x8*)p; }
__device__ __forceinline__ s16x4 ldstr(lds_cptr p) { return __builtin_amdgcn_ds_read_tr16_b64_v4i16((LAS s16x4*)p); }
__device__ __forceinline__ bf16x8 cat8(s16x4 a, s16x4 b) { return __builtin_shufflevector(a, b, 0, 1, 2, 3, 4, 5, 6, 7); }
__device__ __forceinline__ void st_u64(char* p, u64 v) { __hip_atomic_store((gu64*)p, v, __ATOMIC_RELAXED, __HIP_MEMORY_SCOPE_AGENT); }
__device__ __forceinline__ u64 ld_u64(const char* p) { return __hip_atomic_load((gu64*)p, __ATOMIC_RELAXED, __HIP_MEMORY_SCOPE_AGENT); }
constexpr unsigned SPIN_CAP = 1u << 18;
template <int CTRL> __device__ __forceinline__ unsigned dppu(unsigned x) { return (unsigned)__builtin_amdgcn_mov_dpp((int)x, CTRL, 0xf, 0xf, true); }
template <int CTRL> __device__ __forceinline__ float dppf(float x) { return __uint_as_float(dppu<CTRL>(__float_as_uint(x))); }
__device__ __forceinline__ float sum_x16(float x) { auto s = __builtin_amdgcn_permlane16_swap(__float_as_uint(x), __float_as_uint(x), false, false); return __uint_as_float(s[0]) + __uint_as_float(s[1]); }
__device__ __forceinline__ float sum_x32(float x) { auto s = __builtin_amdgcn_permlane32_swap(__float_as_uint(x), __float_as_uint(x), false, false); return __uint_as_float(s[0]) + __uint_as_float(s[1]); }
__device__ __forceinline__ float red32(float a, float b) { auto s = __builtin_amdgcn_permlane32_swap(__float_as_uint(a), __float_as_uint(b), false, false); return __uint_as_float(s[0]) + __uint_as_float(s[1]); }
__device__ __forceinline__ float red16(float a, float b) { auto s = __builtin_amdgcn_permlane16_swap(__float_as_uint(a), __float_as_uint(b), false, false); return __uint_as_float(s[0]) + __uint_as_float(s[1]); }
__device__ __forceinline__ float sum8(float x) { x += dppf<0xB1>(x); x += dppf<0x4E>(x); x += dppf<0x141>(x); return x; }
__device__ __forceinline__ float sum64(float x) { x = sum8(x); x += dppf<0x140>(x); return sum_x32(sum_x16(x)); }
__device__ __forceinline__ unsigned umax64(unsigned x) {
  x = max(x, dppu<0xB1>(x)); x = max(x, dppu<0x4E>(x)); x = max(x, dppu<0x141>(x)); x = max(x, dppu<0x140>(x));
  auto s = __builtin_amdgcn_permlane16_swap(x, x, false, false); x = max(s[0], s[1]);
  auto t = __builtin_amdgcn_permlane32_swap(x, x, false, false); return max(t[0], t[1]);
}
#define STAMP_DECL
#define STAMP(i)
#define CSTAMP(i)
#define STAMP_FLUSH(kid)

constexpr int P1_APITCH = 1168;
constexpr int P1_WBUF = 14336;
constexpr int P1_WOFF = 64 * P1_APITCH;
constexpr int LDS_BYTES = 163840;
static_assert(P1_WOFF + 6 * P1_WBUF <= LDS_BYTES, "P1 LDS");
__device__ __forceinline__ unsigned w_voff(int i, int j) {
  const int R = i / 27, cc = i - R * 27;
  return ((R & ~15) + 4 * (R & 3) + ((R >> 2) & 3)) * 3456 + j * 432 + cc * 16;
}
template <int N> __device__ __forceinline__ void w_dma(const char* wq, int c, int rot, const unsigned (&voff)[2], unsigned dst) {
  int kc = c + rot; kc = kc >= 18 ? kc - 18 : kc;
#pragma unroll
  for (int p = 0; p < N; ++p) glds16(wq + (size_t)kc * (32 * 3456), voff[p], dst + (c % 6) * P1_WBUF + p * 1024);
}
template <int V> __device__ __forceinline__ void wait_vm() { asm volatile("s_waitcnt vmcnt(%0)" :: "n"(V) : "memory"); }
template <int N, bool MM>
__device__ __forceinline__ void p1_main(f32x16 (&acc)[2], lds_cptr ap, lds_cptr bp, const char* wq, int rot, const unsigned (&voff)[2],
                                        unsigned dst, char* ws, unsigned soff) {
  bf16x8 wf[2], xa[2], xb[2];
  auto frags = [&](int c) {
    int kc = c + rot; kc = kc >= 18 ? kc - 18 : kc;
#pragma unroll
    for (int ks = 0; ks < 2; ++ks) {
      wf[ks] = cat8(ldstr(bp + (c % 6) * P1_WBUF + ks * 6912), ldstr(bp + (c % 6) * P1_WBUF + ks * 6912 + 432));
      xa[ks] = lds128(ap + kc * 64 + ks * 32); xb[ks] = lds128(ap + 32 * P1_APITCH + kc * 64 + ks * 32);
    }
  };
  wait_vm<5 * N>();
  LDS_BAR();
  if (MM) frags(0);
#pragma unroll
  for (int c = 0; c < 18; ++c) {
    if (N) { if (c == 0) wait_vm<4 * N>(); else if (c <= 13) wait_vm<3 * N>(); else if (c == 14) wait_vm<2 * N>(); else if (c == 15) wait_vm<N>(); else if (c == 16) wait_vm<0>(); }
    if (c < 17) asm volatile("s_barrier" ::: "memory");
    const bf16x8 w0 = wf[0], w1 = wf[1], a0 = xa[0], a1 = xa[1], b0 = xb[0], b1 = xb[1];
    int kn = c + 1 + rot; kn = kn >= 18 ? kn - 18 : kn;
    const lds_cptr bn = bp + ((c + 1) % 6) * P1_WBUF, an = ap + kn * 64;
    const bool rd = MM && c < 17;
    SB();
    if (MM) acc[0] = MFMA32(w0, a0, acc[0]);
    SB();
    if (rd) { wf[0] = cat8(ldstr(bn), ldstr(bn + 432)); xa[0] = lds128(an); xb[0] = lds128(an + 32 * P1_APITCH); }
    SB();
    if (MM) acc[1] = MFMA32(w0, b0, acc[1]);
    SB();
    if (rd) { wf[1] = cat8(ldstr(bn + 6912), ldstr(bn + 6912 + 432)); xa[1] = lds128(an + 32); xb[1] = lds128(an + 32 * P1_APITCH + 32); }
    SB();
    if (MM) acc[0] = MFMA32(w1, a1, acc[0]);
    SB();
    if (c >= 1 && c <= 12) w_dma<N>(wq, c + 5, rot, voff, dst);
    if (N == 0 && c < 8) { gstore16s(ws + c * 2048, soff, u32x4{SENT, SENT, SENT, SENT}); gstore16s(ws + c * 2048 + 1024, soff, u32x4{SENT, SENT, SENT, SENT}); }
    SB();
    if (MM) acc[1] = MFMA32(w1, b1, acc[1]);
    SB();
  }
}

struct AttnState { f32x16 sA, oA, oB; u32x4 pend; bf16x8 v1p, k1, k1n, v0, v1; s16x4 k2, k2n; };
struct QFrag { bf16x8 q1; s16x4 q2[4]; };
#define EXP2(x) __builtin_amdgcn_exp2f(x)
#define G4(P, i, S, r) do { P[i] = cvtpk(EXP2(S[r]), EXP2(S[(r) + 1])); P[(i) + 1] = cvtpk(EXP2(S[(r) + 2]), EXP2(S[(r) + 3])); } while (0)
__device__ __forceinline__ s16x4 ldk2(lds_cptr p) { return __builtin_bit_cast(s16x4, u32x2{*(const LAS unsigned*)p, *(const LAS unsigned*)(p + 128)}); }
__device__ __forceinline__ bf16x8 ldv(lds_cptr p) { return cat8(ldstr(p), ldstr(p + 320)); }
__device__ __forceinline__ void attn_prime(AttnState& st, lds_cptr kp, lds_cptr k2p, lds_cptr vp, const QFrag& qa) {
  st.k1 = lds128(kp); st.k2 = ldk2(k2p); st.k1n = lds128(kp + 1024); st.v0 = ldv(vp); st.v1 = ldv(vp + 640);
  st.sA = MFMA32K8(st.k2, qa.q2[0], MFMA32(st.k1, qa.q1, f32x16{}));
}
template <int PR, class HOOK>
__device__ __forceinline__ void attn8(AttnState& st, lds_cptr kp, lds_cptr k2p, lds_cptr vp, int nx, const QFrag& qa, const QFrag& qb, HOOK hook) {
#pragma unroll
  for (int t = 0; t < 8; ++t) {
    u32x4 p0, p1; f32x16 sB;
    const int pr = PR == 0 ? ((t & 1) ? -1 : 3 - (t >> 1)) : (t & 3) ? -1 : (PR == 1 ? 3 : 1) - (t >> 2);
    if (pr == 0) __builtin_amdgcn_s_setprio(0);
    if (pr == 1) __builtin_amdgcn_s_setprio(1);
    if (pr == 2) __builtin_amdgcn_s_setprio(2);
    if (pr == 3) __builtin_amdgcn_s_setprio(3);
    __builtin_amdgcn_s_waitcnt(0xC07F);
    const bf16x8 v0n = ldv(t < 7 ? vp + (t + 1) * 1280 : vp + nx), v1n = ldv(t < 7 ? vp + (t + 1) * 1280 + 640 : vp + nx + 640);
    const bf16x8 k1nn = lds128(t < 6 ? kp + (t + 2) * 1024 : kp + nx + (t - 6) * 1024);
    if (t == 2) st.k2n = ldk2(k2p + 512);
    if (t == 6) st.k2n = ldk2(k2p + nx);
    hook(t);
    SB();
    sB = MFMA32(st.k1, qb.q1, f32x16{}); SB();
    G4(p0, 0, st.sA, 0); SB();
    st.oB = MFMA32(st.v1p, __builtin_bit_cast(bf16x8, st.pend), st.oB); SB();
    G4(p0, 2, st.sA, 4); SB();
    sB = MFMA32K8(st.k2, qb.q2[t & 3], sB); SB();
    G4(p1, 0, st.sA, 8); SB();
    st.oA = MFMA32(st.v0, __builtin_bit_cast(bf16x8, p0), st.oA); SB();
    G4(p1, 2, st.sA, 12); SB();
    st.sA = MFMA32(st.k1n, qa.q1, f32x16{}); SB();
    G4(p0, 0, sB, 0); SB();
    st.oA = MFMA32(st.v1, __builtin_bit_cast(bf16x8, p1), st.oA); SB();
    G4(p0, 2, sB, 4); SB();
    if ((t & 3) == 3) st.k2 = st.k2n;
    st.sA = MFMA32K8(st.k2, qa.q2[(t + 1) & 3], st.sA); SB();
    G4(p1, 0, sB, 8); SB();
    st.oB = MFMA32(st.v0, __builtin_bit_cast(bf16x8, p0), st.oB); SB();
    G4(p1, 2, sB, 12); SB();
    st.pend = p1; st.v1p = st.v1; st.k1 = st.k1n; st.k1n = k1nn; st.v0 = v0n; st.v1 = v1n;
  }
  PIN(st.oA); PIN(st.oB); PIN(st.pend); PIN(st.sA);
}
__device__ __forceinline__ int kv_goff(int u) {
  const int slot = u / 19, pc = u - slot * 19;
  return ((slot & 1) * 4 + (slot >> 1)) * CHUNK_B + pc * 1024;
}
constexpr int P2_XOFF = 82944;
constexpr int P3_XOFF = 116736;
constexpr int FB_OFF = 137216;
constexpr int P3_SOFF = 147456;

__global__ void __launch_bounds__(512, 2) k_block(const char* __restrict__ x, const char* __restrict__ wqkv, const char* __restrict__ wo,
                                                  const char* __restrict__ wr, const char* __restrict__ wg, const char* __restrict__ wu,
                                                  const char* __restrict__ wd, char* __restrict__ out, char* __restrict__ ws) {
  extern __shared__ __attribute__((aligned(16))) char smem[];
  const int tid = threadIdx.x, lane = tid & 63, w = __builtin_amdgcn_readfirstlane(tid >> 6);
  const int b = blockIdx.x, xc = b & 7, sl = b >> 3;
  const unsigned lds0 = (unsigned)(uintptr_t)smem;
  const int h = lane >> 5, r32 = lane & 31;
  STAMP_DECL STAMP(0);
  const char* pc0_ = (const char*)__builtin_amdgcn_s_getpc();
  u64 tag;
  {
    const int j = 4 * (xc >> 2) + (sl >> 3), rb = 8 * (xc & 3) + (sl & 7);
    const int row = tid >> 3, j8 = tid & 7;
    u32x4 xr[9];
    {
      const char* xb = x + (size_t)rb * 64 * 1152;
      const unsigned vo = row * 1152 + j8 * 16;
#pragma unroll
      for (int i = 0; i < 9; ++i) xr[i] = gload16(xb + i * 128, vo);
    }
    u32x2 ep = gload8s(ws, WS_EPOCH);
    u32x4 cpf = u32x4{0, 0, 0, 0};
    if (w == 0) cpf = gload16(pc0_, (sl + 32 * (lane >> 5)) * 768 + r32 * 24);
    const int rot = ((sl & 7) * 9) >> 2;
    const int pc0 = 2 * max(w - 1, 0);
    unsigned voff[2];
#pragma unroll
    for (int p = 0; p < 2; ++p) voff[p] = w_voff(min((pc0 + p) * 64 + lane, 863), j);
    const unsigned wdst = lds0 + P1_WOFF + pc0 * 1024;
    asm volatile("s_barrier" ::: "memory");
    if (w) {
#pragma unroll
      for (int c = 0; c < 3; ++c) w_dma<2>(wqkv, c, rot, voff, wdst);
      WAIT_VM(6);
    } else {
      WAIT_VM(0);
    }
#pragma unroll
    for (int i = 0; i < 9; ++i) PIN(xr[i]);
    PIN(ep); PIN(cpf);
    tag = (((u64)ep[1] << 32) | ep[0]) * 6364136223846793005ull + 1442695040888963407ull;
    STAMP(1);
    {
      float s = 0.f, q = 0.f;
#pragma unroll
      for (int i = 0; i < 9; ++i)
#pragma unroll
        for (int e = 0; e < 4; ++e) { s = dot2(xr[i][e], 0x3F803F80u, s); q = dot2(xr[i][e], xr[i][e], q); }
      if (w) {
#pragma unroll
        for (int c = 3; c < 6; ++c) w_dma<2>(wqkv, c, rot, voff, wdst);
      }
      s = sum8(s); q = sum8(q);
      const float m = s * (1.f / 576.f);
      const float var = fmaxf(fmaf(q, 1.f / 576.f, -m * m), 0.f);
      const float rinv = 1.f / rbf(sqrtf(rbf(rbf(var) + 1e-5f)));
      const float nb = -rbf(m) * rinv;
      const lds_ptr ap = (lds_ptr)smem + row * P1_APITCH + j8 * 16;
#pragma unroll
      for (int i = 0; i < 9; ++i) {
        u32x4 o;
#pragma unroll
        for (int e = 0; e < 4; ++e) o[e] = cvtpk(fmaf(bflo(xr[i][e]), rinv, nb), fmaf(bfhi(xr[i][e]), rinv, nb));
        *(LAS u32x4*)(ap + i * 128) = o;
      }
    }
    STAMP(2); CSTAMP(2);
    f32x16 acc[2]; acc[0] = f32x16{}; acc[1] = f32x16{};
    {
      const int sg = 4 * ((lane >> 4) & 1) + (lane & 3);
      const int col = w < 6 ? 36 * w + 4 * ((0x86753120u >> (4 * sg)) & 15) : 36 * min(sg, 5) + 16;
      const lds_cptr ap = (lds_cptr)smem + r32 * P1_APITCH + h * 16;
      const lds_cptr bp = (lds_cptr)smem + P1_WOFF + (4 * ((lane & 15) >> 2) + 2 * h) * 432 + col * 2;
      const unsigned soff = WS_SENT + b * 16384 + lane * 16;
      if (w == 7) p1_main<2, false>(acc, ap, bp, wqkv, rot, voff, wdst, ws, soff);
      else if (w == 0) p1_main<0, true>(acc, ap, bp, wqkv, rot, voff, wdst, ws, soff);
      else p1_main<2, true>(acc, ap, bp, wqkv, rot, voff, wdst, ws, soff);
    }
    STAMP(3);
    constexpr float QS = 0.23570226039551584f * 1.4426950408889634f;
    const int t0 = 2 * rb;
    const int kvt = WS_KV + (t0 >> 3) * CHUNK_B;
    const lds_ptr vst = (lds_ptr)smem + P1_WOFF + (r32 >> 2) * 160;
    if (w < 6) {
      const int P = 6 * j + w, ty = P >> 4, hd = 2 * (P & 15);
      const float sc = ty ? 1.f : QS;
      const int base = (ty ? kvt + hd * HEAD_B + (t0 & 7) * 1024 : WS_Q1 + hd * 65536 + t0 * 1024) + lane * 16;
      const int hs = ty ? HEAD_B : 65536;
#pragma unroll
      for (int mi = 0; mi < 2; ++mi)
#pragma unroll
        for (int ab = 0; ab < 2; ++ab) {
          u32x4 o;
#pragma unroll
          for (int e = 0; e < 4; ++e) o[e] = cvtpk(acc[mi][8 * ab + 2 * e] * sc, acc[mi][8 * ab + 2 * e + 1] * sc);
          if (ty == 2) *(LAS u32x4*)(vst + (2 * w + ab) * 2560 + mi * 1280 + (r32 & 3) * 32 + h * 16) = o;
          else gstore16s(ws, base + ab * hs + mi * 1024, o);
        }
    } else if (w == 6) {
#pragma unroll
      for (int rq = 0; rq < 3; ++rq) {
        const int P = 6 * j + 2 * rq, ty = P >> 4, hd = 2 * ((P + h) & 15);
        const float sc = ty ? 1.f : QS;
#pragma unroll
        for (int mi = 0; mi < 2; ++mi) {
          const unsigned oa = cvtpk(acc[mi][4 * rq] * sc, acc[mi][4 * rq + 1] * sc), ob = cvtpk(acc[mi][4 * rq + 2] * sc, acc[mi][4 * rq + 3] * sc);
          if (ty == 2) {
            const lds_ptr d = vst + (4 * rq + 2 * h) * 2560 + mi * 1280 + 128 + (r32 & 3) * 8;
            *(LAS u32x2*)d = u32x2{oa, 0x00003F80u}; *(LAS u32x2*)(d + 2560) = u32x2{ob, 0x00003F80u};
          } else {
            const int base = (ty ? kvt + hd * HEAD_B + 8192 + (t0 & 7) * 128 : WS_Q2 + hd * 8192 + t0 * 128) + mi * 128 + r32 * 4;
            gstore4s(ws, base, oa); gstore4s(ws, base + (ty ? HEAD_B : 8192), ob);
          }
        }
      }
    }
    if (j >= 5) {
      LDS_BAR();
#pragma unroll
      for (int i = 0; i < 4; ++i) {
        const int pi = i * 512 + tid + (j == 5 ? 640 : 0), hh = pi / 160;
        if (pi < 1920) gstore16s(ws, kvt + (12 * j + hh - 64) * HEAD_B + 9216 + (t0 & 7) * 1280 + (pi - hh * 160) * 16,
                                 *(const LAS u32x4*)(smem + P1_WOFF + pi * 16));
      }
    }
    WAIT_BAR(0);
    if (tid == 0) st_u64(ws + WS_FLAG1 + (j * 32 + rb) * 8, tag);
    STAMP(4); CSTAMP(3);
  }
  const int panel = 4 * xc + (sl & 3), cg = sl >> 2;
  const int mt = w & 3, kq = w >> 2, g = lane >> 4, l16 = lane & 15;
  const int nnt = kq ? 2 : 3, nt0 = kq ? 3 : 0;
  const int tok = panel * 64 + mt * 16 + l16;
  const bool ffn = b < 144;
  const int fs = ((b & 7) + 8 * (b >> 4)) * 2 + ((b >> 3) & 1);
  u32x2 xres[3], wrr[3], gw[9], uw[9];
  unsigned wrow[5];
  u32x4 dw[8];
  {
    const int head = 4 * xc + (sl & 3), qp = sl >> 2;
    const int qpair = w & 3, kh = w >> 2;
    if (w == 0) {
      const char* f1 = ws + WS_FLAG1 + ((h ? (64 + head) / 12 : (32 + head) / 12) * 32 + r32) * 8;
      const char* f2 = ws + WS_FLAG1 + ((head / 12) * 32 + 4 * qp + (lane & 3)) * 8;
      _Pragma("unroll 1") for (unsigned spin = 0; spin < SPIN_CAP; ++spin) {
        const u64 a = ld_u64(f1), c = ld_u64(f2);
        if (__all(a == tag && c == tag)) break;
        __builtin_amdgcn_s_sleep(1);
      }
    }
    LDS_BAR();
    STAMP(5);
    const char* img = ws + WS_KV + (size_t)head * HEAD_B;
    const int qt = qp * 8 + 2 * qpair;
    u32x4 kv[19];
#pragma unroll
    for (int i = 0; i < 5; ++i) kv[i] = gload16s(img + kv_goff(8 * i + w), lane * 16);
    u32x4 q1a = gload16s(ws, WS_Q1 + head * 65536 + qt * 1024 + lane * 16);
    u32x4 q1b = gload16s(ws, WS_Q1 + head * 65536 + qt * 1024 + 1024 + lane * 16);
    unsigned q2a = gload4s(ws, WS_Q2 + head * 8192 + qt * 128 + r32 * 4);
    unsigned q2b = gload4s(ws, WS_Q2 + head * 8192 + qt * 128 + 128 + r32 * 4);
#pragma unroll
    for (int i = 5; i < 8; ++i) kv[i] = gload16s(img + kv_goff(8 * i + w), lane * 16);
    const lds_ptr kdst = (lds_ptr)smem + w * 1024 + lane * 16;
    WAIT_VM(3);
#pragma unroll
    for (int i = 0; i < 5; ++i) { PIN(kv[i]); *(LAS u32x4*)(kdst + i * 8192) = kv[i]; }
    PIN(q1a); PIN(q1b); PIN(q2a); PIN(q2b);
    QFrag qa, qb;
    qa.q1 = __builtin_bit_cast(bf16x8, q1a); qb.q1 = __builtin_bit_cast(bf16x8, q1b);
#pragma unroll
    for (int i = 0; i < 4; ++i) {
      const unsigned ua = h == (i >> 1) ? q2a : 0u, ub = h == (i >> 1) ? q2b : 0u;
      qa.q2[i] = __builtin_bit_cast(s16x4, (i & 1) ? u32x2{0u, ua} : u32x2{ua, 0u});
      qb.q2[i] = __builtin_bit_cast(s16x4, (i & 1) ? u32x2{0u, ub} : u32x2{ub, 0u});
    }
    const int vlo = h * 160 + ((lane & 16) ? 128 + (l16 >> 2) * 8 : l16 * 8);
    const lds_cptr kp = (lds_cptr)smem + kh * CHUNK_B + lane * 16;
    const lds_cptr k2p = (lds_cptr)smem + kh * CHUNK_B + 8192 + h * 256 + r32 * 4;
    const lds_cptr vp = (lds_cptr)smem + kh * CHUNK_B + 9216 + vlo;
    LDS_BAR();
    STAMP(6); CSTAMP(0);
    AttnState st;
    st.oA = f32x16{}; st.oB = f32x16{}; st.pend = u32x4{0, 0, 0, 0}; st.v1p = bf16x8{};
    attn_prime(st, kp, k2p, vp, qa);
    attn8<0>(st, kp, k2p, vp, 0, qa, qb, [&](int t) {
#pragma unroll
      for (int i = 8 + 3 * t; i < 11 + 3 * t; ++i) if (i < 19) kv[i] = gload16s(img + kv_goff(8 * i + w), lane * 16);
    });
    STAMP(14);
    WAIT_VM(0);
#pragma unroll
    for (int i = 5; i < 19; ++i) { PIN(kv[i]); *(LAS u32x4*)(kdst + i * 8192) = kv[i]; }
    LDS_BAR();
    STAMP(15);
    attn_prime(st, kp + 2 * CHUNK_B, k2p + 2 * CHUNK_B, vp + 2 * CHUNK_B, qa);
    const unsigned go = ffn ? (tid >> 3) * (F_ * 2) + fs * 64 + (tid & 7) * 8 : 0u;
    attn8<1>(st, kp + 2 * CHUNK_B, k2p + 2 * CHUNK_B, vp + 2 * CHUNK_B, 2 * CHUNK_B, qa, qb, [&](int t) {
      gw[t] = gload8(wg + t * 64 * (F_ * 2), go);
      if (t == 7) gw[8] = gload8(wg + 8 * 64 * (F_ * 2), go);
      uw[t] = gload8(wu + t * 64 * (F_ * 2), go);
      if (t == 7) uw[8] = gload8(wu + 8 * 64 * (F_ * 2), go);
    });
    STAMP(16);
    const unsigned dn = ffn ? (fs * 32 + g * 8) * 1152 + w * 144 + min(l16, 8) * 16 : 0u;
    attn8<2>(st, kp + 4 * CHUNK_B, k2p + 4 * CHUNK_B, vp + 4 * CHUNK_B, 2 * CHUNK_B, qa, qb, [&](int t) {
      dw[t] = gload16(wd + t * 1152, dn);
      if (t < 3) {
        const int n = min((nt0 + t) * 16 + 4 * g, 68);
        xres[t] = gload8(x, tok * 1152 + (cg * 72 + n) * 2);
        wrr[t] = gload8(wr, (cg * 72 + n) * 2);
      } else {
        wrow[t - 3] = gload4(wr, min((t - 3) * 64 + lane, 287) * 4);
      }
    });
    STAMP(17);
    u32x4 fa, fb;
    LDS_BAR();
    attn8<0>(st, kp + 6 * CHUNK_B, k2p + 6 * CHUNK_B, vp + 6 * CHUNK_B, 0, qa, qb, [&](int t) {
#pragma unroll
      for (int i = 3 * t; i < 3 * t + 3; ++i) {
        if (i < 11) {
          const int pc = i < 10 ? 8 * i + w : 80;
          const int ci = pc * 64 + lane;
          const int R = ci / 9, cc = ci - R * 9;
          glds16(wo, R * 1152 + cg * 144 + cc * 16, lds0 + pc * 1024);
        }
      }
      if (t == 4) { fa = gload16s(ws, WS_FLAG1 + lane * 32); fb = gload16s(ws, WS_FLAG1 + lane * 32 + 16); }
    });
    st.oB = MFMA32(st.v1p, __builtin_bit_cast(bf16x8, st.pend), st.oB);
    STAMP(7); CSTAMP(1);
    LAS f32x4* xw = (LAS f32x4*)(smem + P2_XOFF) + (qpair * 2 + kh) * 192 + lane;
    LAS f32x4* xr_ = (LAS f32x4*)(smem + P2_XOFF) + (qpair * 2 + (kh ^ 1)) * 192 + lane;
    f32x16 o = kh ? st.oB : st.oA;
    {
      const f32x16 give = kh ? st.oA : st.oB;
#pragma unroll
      for (int r = 0; r < 3; ++r) xw[r * 64] = f32x4{give[4 * r], give[4 * r + 1], give[4 * r + 2], give[4 * r + 3]};
    }
    WAIT_BAR(0);
#pragma unroll
    for (int r = 0; r < 3; ++r) {
      const f32x4 t = xr_[r * 64];
#pragma unroll
      for (int e = 0; e < 4; ++e) o[4 * r + e] += t[e];
    }
    PIN(fa); PIN(fb);
    _Pragma("unroll 1") for (unsigned spin = 0; spin < SPIN_CAP; ++spin) {
      const unsigned t0 = (unsigned)tag, t1 = (unsigned)(tag >> 32);
      if (__all(fa[0] == t0 && fa[1] == t1 && fa[2] == t0 && fa[3] == t1 && fb[0] == t0 && fb[1] == t1 && fb[2] == t0 && fb[3] == t1)) break;
      fa = gload16s_w(ws, WS_FLAG1 + lane * 32); fb = gload16s_w(ws, WS_FLAG1 + lane * 32 + 16);
    }
    float l;
    {
      auto sw = __builtin_amdgcn_permlane32_swap(__float_as_uint(o[10]), __float_as_uint(o[10]), false, false);
      l = __uint_as_float(sw[0]);
    }
    bool ok = (l > 8.6736174e-19f) && (l < 1.1529215e18f);
#pragma unroll
    for (int r = 0; r < 10; ++r) ok = ok && ((h && r >= 8) || fabsf(o[r]) < 3.0e38f);
    if (!__all(ok)) {
      const bf16x8 qm1 = kh ? qb.q1 : qa.q1; const s16x4 qm2 = kh ? qb.q2[0] : qa.q2[0];
      const lds_ptr fb = (lds_ptr)smem + FB_OFF + w * 1280;
      float mx = 0.f;
#pragma unroll 1
      for (int pass = 0; pass < 2; ++pass) {
        f32x16 cin;
#pragma unroll
        for (int r = 0; r < 16; ++r) cin[r] = -mx;
        float m2 = -INFINITY;
        o = f32x16{};
#pragma unroll 1
        for (int kt = 0; kt < 64; ++kt) {
          const char* cb = img + (kt >> 3) * CHUNK_B;
          u32x4 k1 = gload16s(cb + (kt & 7) * 1024, lane * 16);
          unsigned k2 = gload4s(cb + 8192 + (kt & 7) * 128, r32 * 4);
          u32x4 va = gload16s(cb + 9216 + (kt & 7) * 1280, lane * 16), vb = gload16s(cb + 9216 + (kt & 7) * 1280 + 1024, l16 * 16);
          WAIT_VM(0); PIN(k1); PIN(k2); PIN(va); PIN(vb);
          *(LAS u32x4*)(fb + lane * 16) = va; *(LAS u32x4*)(fb + 1024 + l16 * 16) = vb;
          f32x16 s = MFMA32(__builtin_bit_cast(bf16x8, k1), qm1, cin);
          s = MFMA32K8(__builtin_bit_cast(s16x4, (u32x2{k2, 0u})), qm2, s);
#pragma unroll
          for (int r = 0; r < 16; ++r) m2 = fmaxf(m2, s[r]);
          u32x4 p0, p1;
          G4(p0, 0, s, 0); G4(p0, 2, s, 4); G4(p1, 0, s, 8); G4(p1, 2, s, 12);
          asm volatile("s_waitcnt lgkmcnt(0)" ::: "memory");
          o = MFMA32(ldv((lds_cptr)fb + vlo), __builtin_bit_cast(bf16x8, p0), o);
          o = MFMA32(ldv((lds_cptr)fb + vlo + 640), __builtin_bit_cast(bf16x8, p1), o);
          asm volatile("s_waitcnt lgkmcnt(0)" ::: "memory");
        }
        auto sw = __builtin_amdgcn_permlane32_swap(__float_as_uint(m2), __float_as_uint(m2), false, false);
        mx = fmaxf(__uint_as_float(sw[0]), __uint_as_float(sw[1]));
      }
      auto sw = __builtin_amdgcn_permlane32_swap(__float_as_uint(o[10]), __float_as_uint(o[10]), false, false);
      l = __uint_as_float(sw[0]);
    }
    const float rl = 1.f / l;
    u32x4 ov;
#pragma unroll
    for (int e = 0; e < 4; ++e) ov[e] = nsent(cvtpk(o[2 * e] * rl, o[2 * e + 1] * rl));
    gstore16s(ws, WS_O1 + head * 65536 + (qt + kh) * 1024 + lane * 16, ov);
    if (!h) gstore4s(ws, WS_O2 + head * 8192 + ((qt + kh) * 32 + r32) * 4, nsent(cvtpk(o[8] * rl, o[9] * rl)));
    STAMP(8);
  }
  float wsum;
  {
#pragma unroll
    for (int i = 0; i < 5; ++i) PIN(wrow[i]);
    if (h) wrow[4] = 0u;
    float t = 0.f;
#pragma unroll
    for (int i = 0; i < 5; ++i) t = dot2(wrow[i], 0x3F803F80u, t);
    wsum = sum64(t);
  }
  f32x4 acc[5];
  {
    u32x4 of[9];
    const unsigned o1 = WS_O1 + (kq * 18 + (g >> 1)) * 65536 + (2 * panel + (mt >> 1)) * 1024 + (g & 1) * 512 + ((mt & 1) * 16 + l16) * 16;
    const int q = l16 >> 2;
    const lds_cptr bp = (lds_cptr)smem + (kq * 9 * 36 + 20 * (g >> 1) + 4 * (g & 1) + q) * 144 + (lane & 3) * 8;
    const lds_cptr bp2 = (lds_cptr)smem + (18 * (4 * g + (q >> 1)) + ((q >> 1) ? 0 : 16) + (q & 1)) * 144 + (lane & 3) * 8;
    bf16x8 wpf[25];
#pragma unroll
    for (int i = 0; i < 25; ++i) {
      wpf[i] = cat8(ldstr(bp + (i / 5) * (36 * 144) + (i % 5) * 32), ldstr(bp + (i / 5) * (36 * 144) + 8 * 144 + (i % 5) * 32));
      PIN(wpf[i]);
    }
#pragma unroll
    for (int i = 0; i < 9; ++i) of[i] = u32x4{SENT, SENT, SENT, SENT};
    if (kq == 0) {
      _Pragma("unroll 1") for (unsigned spin = 0; spin < SPIN_CAP; ++spin) {
        poll_o9(of, ws, o1);
        unsigned m = 0;
#pragma unroll
        for (int i = 0; i < 9; ++i) m = max(m, mx4(of[i]));
        if (__all(m != SENT)) break;
        __builtin_amdgcn_s_sleep(1);
      }
    } else {
      unsigned d[8] = {SENT, SENT, SENT, SENT, SENT, SENT, SENT, SENT};
      _Pragma("unroll 1") for (unsigned spin = 0; spin < SPIN_CAP; ++spin) {
        poll_o7(of, d, ws, o1, WS_O2 + g * 32768 + tok * 4);
        of[7] = u32x4{d[0], d[1], d[2], d[3]}; of[8] = u32x4{d[4], d[5], d[6], d[7]};
        unsigned m = 0;
#pragma unroll
        for (int i = 0; i < 9; ++i) m = max(m, mx4(of[i]));
        if (__all(m != SENT)) break;
        __builtin_amdgcn_s_sleep(1);
      }
    }
    STAMP(9);
#pragma unroll
    for (int i = 0; i < 5; ++i) acc[i] = f32x4{};
#pragma unroll
    for (int ks = 0; ks < 9; ++ks) {
      const bool sp = ks >= 7 && kq;
      const lds_cptr b0 = ks >= 7 ? (sp ? bp2 + (ks - 7) * (288 * 144) : bp + ks * (36 * 144)) : bp + ks * (36 * 144);
      const int d2 = sp ? 36 * 144 : 8 * 144;
#pragma unroll
      for (int i = 0; i < 5; ++i) {
        const bf16x8 wf = ks < 5 ? wpf[ks * 5 + i] : cat8(ldstr(b0 + i * 32), ldstr(b0 + d2 + i * 32));
        acc[i] = MFMA16(wf, __builtin_bit_cast(bf16x8, of[ks]), acc[i]);
      }
    }
    LAS f32x4* xw = (LAS f32x4*)(smem + P3_XOFF) + mt * 320 + lane;
    if (kq == 0) { xw[3 * 64] = acc[3]; xw[4 * 64] = acc[4]; } else { xw[0] = acc[0]; xw[64] = acc[1]; xw[128] = acc[2]; }
    LDS_BAR();
    if (kq == 0) { acc[0] += xw[0]; acc[1] += xw[64]; acc[2] += xw[128]; }
    else { acc[0] = acc[3] + xw[3 * 64]; acc[1] = acc[4] + xw[4 * 64]; acc[2] = f32x4{}; }
  }
#pragma unroll
  for (int i = 0; i < 3; ++i) { PIN(xres[i]); PIN(wrr[i]); }
  STAMP(10);
  const int rgp = panel * 4 + mt;
  float hv[3][4];
  unsigned key; float mb, rinv;
  {
    float s1 = 0.f, s2 = 0.f, s3 = 0.f;
#pragma unroll
    for (int i = 0; i < 3; ++i) {
      const bool valid = (i < nnt) && ((nt0 + i) * 16 + 4 * g < 72);
      const unsigned a01 = cvtpk(acc[i][0], acc[i][1]), a23 = cvtpk(acc[i][2], acc[i][3]);
      unsigned h01 = cvtpk(bflo(xres[i][0]) + bflo(a01), bfhi(xres[i][0]) + bfhi(a01));
      unsigned h23 = cvtpk(bflo(xres[i][1]) + bflo(a23), bfhi(xres[i][1]) + bfhi(a23));
      h01 = valid ? h01 : 0u; h23 = valid ? h23 : 0u;
      hv[i][0] = bflo(h01); hv[i][1] = bfhi(h01); hv[i][2] = bflo(h23); hv[i][3] = bfhi(h23);
      s1 += (hv[i][0] + hv[i][1]) + (hv[i][2] + hv[i][3]);
      s2 = fmaf(hv[i][0], hv[i][0], fmaf(hv[i][1], hv[i][1], fmaf(hv[i][2], hv[i][2], fmaf(hv[i][3], hv[i][3], s2))));
      s3 = fmaf(hv[i][0], bflo(wrr[i][0]), fmaf(hv[i][1], bfhi(wrr[i][0]), fmaf(hv[i][2], bflo(wrr[i][1]), fmaf(hv[i][3], bfhi(wrr[i][1]), s3))));
    }
    s1 = sum_x32(sum_x16(s1)); s2 = sum_x32(sum_x16(s2)); s3 = sum_x32(sum_x16(s3));
    if (g == 0) gstore16s(ws, SY_STATS + rgp * 4096 + (cg * 2 + kq) * 256 + l16 * 16,
                          u32x4{nsent(__float_as_uint(s1)), nsent(__float_as_uint(s2)), nsent(__float_as_uint(s3)), 0u});
    u32x4 sv[4];
#pragma unroll
    for (int i = 0; i < 4; ++i) sv[i] = u32x4{SENT, SENT, SENT, SENT};
    _Pragma("unroll 1") for (unsigned spin = 0; spin < SPIN_CAP; ++spin) {
      poll_4x16(sv, ws, SY_STATS + rgp * 4096 + lane * 16);
      if (__all(max(max(mx4(sv[0]), mx4(sv[1])), max(mx4(sv[2]), mx4(sv[3]))) != SENT)) break;
      __builtin_amdgcn_s_sleep(1);
    }
    STAMP(11);
#define SUMC(c) sum_x32(sum_x16((__uint_as_float(sv[0][c]) + __uint_as_float(sv[1][c])) + (__uint_as_float(sv[2][c]) + __uint_as_float(sv[3][c]))))
    s1 = SUMC(0); s2 = SUMC(1); s3 = SUMC(2);
    const float m = s1 * (1.f / 576.f);
    mb = rbf(m);
    rinv = 1.f / rbf(sqrtf(rbf(rbf(fmaxf(fmaf(s2, 1.f / 576.f, -m * m), 0.f)) + 1e-5f)));
    const unsigned u = __float_as_uint((s3 - mb * wsum) * rinv);
    const unsigned ord = (u & 0x80000000u) ? ~u : (u | 0x80000000u);
    key = (min(ord, 0xFFFFF000u) & ~0x7FFu) | (unsigned)(2047 - tok);
    key = max(key, dppu<0xB1>(key)); key = max(key, dppu<0x4E>(key)); key = max(key, dppu<0x141>(key)); key = max(key, dppu<0x140>(key));
  }
  if (cg == 0 && kq == 0 && lane < 16) gstore4s(ws, SY_BEST + lane * 4096 + rgp * 4, key);
  const bool isbest = tok == 2047 - (int)(key & 0x7FFu);
  u32x2 ov[3];
#pragma unroll
  for (int i = 0; i < 3; ++i) {
    const bool valid = (i < nnt) && ((nt0 + i) * 16 + 4 * g < 72);
    const unsigned d01 = cvtpk(hv[i][0] - mb, hv[i][1] - mb), d23 = cvtpk(hv[i][2] - mb, hv[i][3] - mb);
    const unsigned n01 = cvtpk(bflo(d01) * rinv, bfhi(d01) * rinv), n23 = cvtpk(bflo(d23) * rinv, bfhi(d23) * rinv);
    ov[i] = u32x2{cvtpk(hv[i][0] + bflo(n01), hv[i][1] + bfhi(n01)), cvtpk(hv[i][2] + bflo(n23), hv[i][3] + bfhi(n23))};
    const int col = cg * 72 + (nt0 + i) * 16 + 4 * g;
    if (valid && isbest) {
      gstore8s(ws, SY_CHN + rgp * 1152 + col * 2, u32x2{nsent(n01), nsent(n23)});
      gstore8s(ws, SY_CH + rgp * 1152 + col * 2, u32x2{nsent(cvtpk(hv[i][0], hv[i][1])), nsent(cvtpk(hv[i][2], hv[i][3]))});
    }
  }
  float gf[9][8], df[8][8];
#pragma unroll
  for (int i = 0; i < 9; ++i) {
    PIN(gw[i]); PIN(uw[i]);
    gf[i][0] = bflo(gw[i][0]); gf[i][1] = bfhi(gw[i][0]); gf[i][2] = bflo(gw[i][1]); gf[i][3] = bfhi(gw[i][1]);
    gf[i][4] = bflo(uw[i][0]); gf[i][5] = bfhi(uw[i][0]); gf[i][6] = bflo(uw[i][1]); gf[i][7] = bfhi(uw[i][1]);
  }
#pragma unroll
  for (int i = 0; i < 8; ++i) {
    PIN(dw[i]);
#pragma unroll
    for (int e = 0; e < 4; ++e) { df[i][2 * e] = bflo(dw[i][e]); df[i][2 * e + 1] = bfhi(dw[i][e]); }
  }
#pragma unroll
  for (int i = 0; i < 72; ++i) PIN(gf[i >> 3][i & 7]);
#pragma unroll
  for (int i = 0; i < 64; ++i) PIN(df[i >> 3][i & 7]);
  LAS unsigned* xs = (LAS unsigned*)(smem + P3_SOFF + 8192);
  if (w == 0) {
    u32x2 a = u32x2{SENT, SENT};
    _Pragma("unroll 1") for (unsigned spin = 0; spin < SPIN_CAP; ++spin) {
      poll_8(a, ws, SY_BEST + (b & 15) * 4096 + lane * 8);
      if (__all(max(a[0], a[1]) != SENT)) break;
      __builtin_amdgcn_s_sleep(1);
    }
    const int s0 = __builtin_amdgcn_readfirstlane(2047 - (int)(umax64(max(a[0], a[1])) & 0x7FFu));
    STAMP(12);
    if (b < 240) {
      unsigned xv[5] = {SENT, SENT, SENT, SENT, SENT};
      _Pragma("unroll 1") for (unsigned spin = 0; spin < SPIN_CAP; ++spin) {
        poll_5x4(xv, ws, SY_CHN + (s0 >> 4) * 1152 + lane * 4, SY_CHN + (s0 >> 4) * 1152 + 1024 + r32 * 4);
        unsigned m = 0;
#pragma unroll
        for (int i = 0; i < 5; ++i) m = max(m, xv[i]);
        if (__all(m != SENT)) break;
      }
#pragma unroll
      for (int i = 0; i < 5; ++i) { xs[i * 64 + lane] = xv[i]; *(LAS u32x2*)(xs + 384 + i * 128 + lane * 2) = u32x2{xv[i] << 16, xv[i] & 0xffff0000u}; }
    }
    xs[320] = s0;
  }
  LDS_BAR();
  const int sel = __builtin_amdgcn_readfirstlane(xs[320]);
  const int wgrp = sel >> 4;
  if (ffn) {
    STAMP(13);
    float ga[8] = {0.f, 0.f, 0.f, 0.f, 0.f, 0.f, 0.f, 0.f};
#pragma unroll
    for (int i = 0; i < 9; ++i) {
      const float xf = __uint_as_float(xs[384 + (tid >> 3) + 64 * i]);
#pragma unroll
      for (int e = 0; e < 8; ++e) ga[e] = fmaf(xf, gf[i][e], ga[e]);
    }
    LAS float* gp = (LAS float*)(smem + P3_SOFF);
    {
      const float r0 = red32(ga[0], ga[4]), r1 = red32(ga[1], ga[5]), r2 = red32(ga[2], ga[6]), r3 = red32(ga[3], ga[7]);
      *(LAS u32x2*)(gp + (w * 2 + ((lane >> 3) & 1)) * 64 + h * 32 + (lane & 7) * 4 + ((lane >> 4) & 1) * 2) =
          u32x2{__float_as_uint(red16(r0, r2)), __float_as_uint(red16(r1, r3))};
    }
    LDS_BAR();
    float av;
    {
      float s = 0.f;
#pragma unroll
      for (int i = 0; i < 16; ++i) s += gp[i * 64 + lane];
      s = rbf(s);
      auto sw = __builtin_amdgcn_permlane32_swap(__float_as_uint(s), __float_as_uint(s), false, false);
      const float gs = __uint_as_float(sw[0]), us = __uint_as_float(sw[1]);
      av = rbf(rbf(gs / (1.f + __expf(-gs))) * us);
    }
    float y[8] = {0.f, 0.f, 0.f, 0.f, 0.f, 0.f, 0.f, 0.f};
#pragma unroll
    for (int i = 0; i < 8; ++i) {
      const float a = __shfl(av, g * 8 + i);
#pragma unroll
      for (int e = 0; e < 8; ++e) y[e] = fmaf(a, df[i][e], y[e]);
    }
    const float r0 = red32(y[0], y[4]), r1 = red32(y[1], y[5]), r2 = red32(y[2], y[6]), r3 = red32(y[3], y[7]);
    const float y0 = red16(r0, r2), y1 = red16(r1, r3);
    if (l16 < 9) gstore8s(ws, SY_PART + fs * 2304 + (w * 72 + l16 * 8 + g * 2) * 4, u32x2{nsent(__float_as_uint(y0)), nsent(__float_as_uint(y1))});
  }
  if (tok != sel) {
#pragma unroll
    for (int i = 0; i < 3; ++i) {
      const bool valid = (i < nnt) && ((nt0 + i) * 16 + 4 * g < 72);
      if (valid) *(u32x2*)(out + (size_t)tok * 1152 + (cg * 72 + (nt0 + i) * 16 + 4 * g) * 2) = ov[i];
    }
  }
  if (!ffn && b < 240 && w < 3) {
    const int cp = (b - 144) * 3 + w;
    float r = 0.f;
#pragma unroll
    for (int i = 0; i < 5; ++i) { const unsigned xv = xs[i * 64 + lane]; r = fmaf(bflo(xv), bflo(wrow[i]), fmaf(bfhi(xv), bfhi(wrow[i]), r)); }
    const unsigned np = xs[cp];
    r = rbf(sum64(r));
    const float sig = rbf(1.f / (1.f + __expf(-r)));
    STAMP(13);
    u32x2 pv[3] = {u32x2{SENT, SENT}, u32x2{SENT, SENT}, u32x2{SENT, SENT}};
    unsigned hp = SENT;
    _Pragma("unroll 1") for (unsigned spin = 0; spin < SPIN_CAP; ++spin) {
      poll_part(pv, hp, ws, SY_PART + lane * 2304 + cp * 8, SY_PART + (64 + lane) * 2304 + cp * 8, SY_PART + (128 + l16) * 2304 + cp * 8,
                SY_CH + wgrp * 1152 + cp * 4);
      unsigned m = hp;
#pragma unroll
      for (int i = 0; i < 3; ++i) m = max(m, max(pv[i][0], pv[i][1]));
      if (__all(m != SENT)) break;
      __builtin_amdgcn_s_sleep(1);
    }
    const bool v2 = lane < 16;
    const float y0 = sum64(__uint_as_float(pv[0][0]) + __uint_as_float(pv[1][0]) + (v2 ? __uint_as_float(pv[2][0]) : 0.f));
    const float y1 = sum64(__uint_as_float(pv[0][1]) + __uint_as_float(pv[1][1]) + (v2 ? __uint_as_float(pv[2][1]) : 0.f));
    const float r0 = bflo(hp) + rbf(bflo(np) + rbf(rbf(y0) * sig));
    const float r1 = bfhi(hp) + rbf(bfhi(np) + rbf(rbf(y1) * sig));
    if (lane == 0) *(unsigned*)(out + (size_t)sel * 1152 + cp * 4) = cvtpk(r0, r1);
    if (b == 144 && tid == 0) st_u64(ws + WS_EPOCH, tag);
  }
  STAMP_FLUSH(0);
}

extern "C" void kernel_launch(void* const* d_in, const int* in_sizes, int n_in, void* d_out, int out_size, void* d_ws,
                              size_t ws_size, hipStream_t stream) {
  (void)hipFuncSetAttribute((const void*)k_block, hipFuncAttributeMaxDynamicSharedMemorySize, LDS_BYTES);
  k_block<<<256, 512, LDS_BYTES, stream>>>((const char*)d_in[0], (const char*)d_in[1], (const char*)d_in[2], (const char*)d_in[4],
                                           (const char*)d_in[5], (const char*)d_in[6], (const char*)d_in[7], (char*)d_out, (char*)d_ws);
}
```

```cpp
#include <hip/hip_runtime.h>
#include <hip/hip_bf16.h>

typedef __attribute__((ext_vector_type(8))) short bf16x8;
typedef __attribute__((ext_vector_type(4))) short s16x4;
typedef __attribute__((ext_vector_type(16))) float f32x16;
typedef __attribute__((ext_vector_type(4))) float f32x4;
typedef __attribute__((ext_vector_type(4))) unsigned u32x4;
typedef __attribute__((ext_vector_type(2))) unsigned u32x2;
typedef __attribute__((ext_vector_type(2))) __bf16 b2_t;
typedef unsigned long long u64;
#define LAS __attribute__((address_space(3)))
#define GAS __attribute__((address_space(1)))
typedef LAS const char* lds_cptr;
typedef LAS char* lds_ptr;
typedef GAS u64 gu64;
typedef GAS unsigned gu32;

constexpr int S_ = 2048, D_ = 576, H_ = 32, F_ = 4608;
constexpr int CHUNK_B = 19456;
constexpr int HEAD_B = 8 * CHUNK_B;
constexpr int WS_EPOCH = 0;
constexpr int WS_FLAG1 = 4096;
constexpr int WS_KV = 8192;
constexpr int WS_Q1 = WS_KV + H_ * HEAD_B;
constexpr int WS_Q2 = WS_Q1 + H_ * 65536;
constexpr int WS_SENT = WS_Q2 + H_ * 8192;
constexpr int WS_O1 = WS_SENT;
constexpr int WS_O2 = WS_O1 + H_ * 65536;
constexpr int SY_STATS = WS_O2 + S_ * 128;
constexpr int SY_BEST = SY_STATS + 128 * 4096;
constexpr int SY_CHN = SY_BEST + 16 * 4096;
constexpr int SY_CH = SY_CHN + 128 * 1152;
constexpr int SY_PART = SY_CH + 128 * 1152;
constexpr int SY_END = SY_PART + 144 * 2304;
static_assert(SY_END - WS_SENT <= 256 * 16384, "sentinel area");
constexpr unsigned SENT = 0xFFFFFFFFu;

#define MFMA32(a, b, c) __builtin_amdgcn_mfma_f32_32x32x16_bf16(a, b, c, 0, 0, 0)
#define MFMA32K8(a, b, c) __builtin_amdgcn_mfma_f32_32x32x8bf16_1k(a, b, c, 0, 0, 0)
#define MFMA16(a, b, c) __builtin_amdgcn_mfma_f32_16x16x32_bf16(a, b, c, 0, 0, 0)
#define WAIT_VM(N) asm volatile("s_waitcnt vmcnt(" #N ")" ::: "memory")
#define WAIT_BAR(N) asm volatile("s_waitcnt vmcnt(" #N ") lgkmcnt(0)\n\ts_barrier" ::: "memory")
#define LDS_BAR() asm volatile("s_waitcnt lgkmcnt(0)\n\ts_barrier" ::: "memory")
#define PIN(x) asm volatile("" : "+v"(x))
#define SB() __builtin_amdgcn_sched_barrier(0)

__device__ __forceinline__ unsigned cvtpk(float lo, float hi) {
  typedef __attribute__((ext_vector_type(2))) float f2_t;
  return __builtin_bit_cast(unsigned, __builtin_convertvector((f2_t{lo, hi}), b2_t));
}
__device__ __forceinline__ float bflo(unsigned u) { return __uint_as_float(u << 16); }
__device__ __forceinline__ float bfhi(unsigned u) { return __uint_as_float(u & 0xffff0000u); }
__device__ __forceinline__ float rbf(float x) { return bflo(cvtpk(x, x)); }
__device__ __forceinline__ float dot2(unsigned a, unsigned b, float c) {
  return __builtin_amdgcn_fdot2_f32_bf16(__builtin_bit_cast(b2_t, a), __builtin_bit_cast(b2_t, b), c, false);
}
__device__ __forceinline__ void glds16(const void* sbase, unsigned voff, unsigned lds_dst) {
  unsigned sv;
  asm volatile("s_mov_b32 %0, m0\n\ts_mov_b32 m0, %3\n\ts_nop 0\n\tglobal_load_lds_dwordx4 %1, %2\n\ts_mov_b32 m0, %0"
               : "=&s"(sv) : "v"(voff), "s"(sbase), "s"(lds_dst) : "memory");
}
__device__ __forceinline__ u32x4 gload16(const void* sbase, unsigned voff) {
  u32x4 r; asm volatile("global_load_dwordx4 %0, %1, %2" : "=v"(r) : "v"(voff), "s"(sbase) : "memory"); return r;
}
__device__ __forceinline__ u32x2 gload8(const void* sbase, unsigned voff) {
  u32x2 r; asm volatile("global_load_dwordx2 %0, %1, %2" : "=v"(r) : "v"(voff), "s"(sbase) : "memory"); return r;
}
__device__ __forceinline__ u32x4 gload16s(const void* sbase, unsigned voff) {
  u32x4 r; asm volatile("global_load_dwordx4 %0, %1, %2 sc1" : "=v"(r) : "v"(voff), "s"(sbase) : "memory"); return r;
}
__device__ __forceinline__ u32x2 gload8s(const void* sbase, unsigned voff) {
  u32x2 r; asm volatile("global_load_dwordx2 %0, %1, %2 sc1" : "=v"(r) : "v"(voff), "s"(sbase) : "memory"); return r;
}
__device__ __forceinline__ unsigned gload4s(const void* sbase, unsigned voff) {
  unsigned r; asm volatile("global_load_dword %0, %1, %2 sc1" : "=v"(r) : "v"(voff), "s"(sbase) : "memory"); return r;
}
__device__ __forceinline__ void gstore16s(void* sbase, unsigned voff, u32x4 v) {
  asm volatile("global_store_dwordx4 %0, %1, %2 sc1\n\ts_nop 1" :: "v"(voff), "v"(v), "s"(sbase) : "memory");
}
__device__ __forceinline__ void gstore4s(void* sbase, unsigned voff, unsigned v) {
  asm volatile("global_store_dword %0, %1, %2 sc1" :: "v"(voff), "v"(v), "s"(sbase) : "memory");
}
__device__ __forceinline__ void gstore8s(void* sbase, unsigned voff, u32x2 v) {
  asm volatile("global_store_dwordx2 %0, %1, %2 sc1" :: "v"(voff), "v"(v), "s"(sbase) : "memory");
}
__device__ __forceinline__ unsigned gload4(const void* sbase, unsigned voff) {
  unsigned r; asm volatile("global_load_dword %0, %1, %2" : "=v"(r) : "v"(voff), "s"(sbase) : "memory"); return r;
}
__device__ __forceinline__ unsigned nsent(unsigned v) { return min(v, SENT - 1u); }
__device__ __forceinline__ unsigned mx4(u32x4 v) { return max(max(v[0], v[1]), max(v[2], v[3])); }
#define PL(W, R, M, A) "v_cmp_eq_u32 vcc, -1, %[" M "]\n\ts_and_saveexec_b64 %[sv], vcc\n\tglobal_load_" W " %[" R "], %[" A "], %[b] sc1\n\ts_mov_b64 exec, %[sv]\n\t"
#define PADD(N) "v_add_u32 %[t], " N ", %[t]\n\t"
#define PEND "s_waitcnt vmcnt(0)"
#define PL7 PL("dwordx4", "r0", "m0", "t") PADD("0x20000") PL("dwordx4", "r1", "m1", "t") PADD("0x20000") PL("dwordx4", "r2", "m2", "t") PADD("0x20000") \
            PL("dwordx4", "r3", "m3", "t") PADD("0x20000") PL("dwordx4", "r4", "m4", "t") PADD("0x20000") PL("dwordx4", "r5", "m5", "t") PADD("0x20000") \
            PL("dwordx4", "r6", "m6", "t")
#define PL7O [r0] "+v"(r[0]), [r1] "+v"(r[1]), [r2] "+v"(r[2]), [r3] "+v"(r[3]), [r4] "+v"(r[4]), [r5] "+v"(r[5]), [r6] "+v"(r[6]), [sv] "=&s"(sv), [t] "+v"(a0)
#define PL7I [m0] "v"(mx4(r[0])), [m1] "v"(mx4(r[1])), [m2] "v"(mx4(r[2])), [m3] "v"(mx4(r[3])), [m4] "v"(mx4(r[4])), [m5] "v"(mx4(r[5])), \
             [m6] "v"(mx4(r[6])), [b] "s"(sb)
__device__ __forceinline__ void poll_o9(u32x4 (&r)[9], const void* sb, unsigned a0) {
  u64 sv;
  asm volatile(PL7 PADD("0x20000") PL("dwordx4", "r7", "m7", "t") PADD("0x20000") PL("dwordx4", "r8", "m8", "t") PEND
               : PL7O, [r7] "+v"(r[7]), [r8] "+v"(r[8]) : PL7I, [m7] "v"(mx4(r[7])), [m8] "v"(mx4(r[8])) : "vcc", "memory");
}
#define PLD(R) PL("dword", R, R, "u")
__device__ __forceinline__ void poll_o7(u32x4 (&r)[9], unsigned (&d)[8], const void* sb, unsigned a0, unsigned a2) {
  u64 sv;
  asm volatile(PL7 PLD("d0") "v_add_u32 %[u], 0x2000, %[u]\n\t" PLD("d1") "v_add_u32 %[u], 0x2000, %[u]\n\t" PLD("d2") "v_add_u32 %[u], 0x2000, %[u]\n\t"
               PLD("d3") "v_add_u32 %[u], 0x1a000, %[u]\n\t" PLD("d4") "v_add_u32 %[u], 0x2000, %[u]\n\t" PLD("d5") "v_add_u32 %[u], 0x2000, %[u]\n\t"
               PLD("d6") "v_add_u32 %[u], 0x2000, %[u]\n\t" PLD("d7") PEND
               : PL7O, [u] "+v"(a2), [d0] "+v"(d[0]), [d1] "+v"(d[1]), [d2] "+v"(d[2]), [d3] "+v"(d[3]), [d4] "+v"(d[4]), [d5] "+v"(d[5]),
                 [d6] "+v"(d[6]), [d7] "+v"(d[7]) : PL7I : "vcc", "memory");
}
__device__ __forceinline__ void poll_4x16(u32x4 (&r)[4], const void* sb, unsigned a0) {
  u64 sv;
  asm volatile(PL("dwordx4", "r0", "m0", "t") PADD("0x400") PL("dwordx4", "r1", "m1", "t") PADD("0x400") PL("dwordx4", "r2", "m2", "t") PADD("0x400")
               PL("dwordx4", "r3", "m3", "t") PEND
               : [r0] "+v"(r[0]), [r1] "+v"(r[1]), [r2] "+v"(r[2]), [r3] "+v"(r[3]), [sv] "=&s"(sv), [t] "+v"(a0)
               : [m0] "v"(mx4(r[0])), [m1] "v"(mx4(r[1])), [m2] "v"(mx4(r[2])), [m3] "v"(mx4(r[3])), [b] "s"(sb) : "vcc", "memory");
}
__device__ __forceinline__ void poll_8(u32x2& r, const void* sb, unsigned a0) {
  u64 sv;
  asm volatile(PL("dwordx2", "r0", "m0", "a0") PEND
               : [r0] "+v"(r), [sv] "=&s"(sv) : [m0] "v"(max(r[0], r[1])), [a0] "v"(a0), [b] "s"(sb) : "vcc", "memory");
}
__device__ __forceinline__ void poll_5x4(unsigned (&r)[5], const void* sb, unsigned a0, unsigned a4) {
  u64 sv;
  asm volatile(PL("dword", "r0", "r0", "t") PADD("0x100") PL("dword", "r1", "r1", "t") PADD("0x100") PL("dword", "r2", "r2", "t") PADD("0x100")
               PL("dword", "r3", "r3", "t") PL("dword", "r4", "r4", "a4") PEND
               : [r0] "+v"(r[0]), [r1] "+v"(r[1]), [r2] "+v"(r[2]), [r3] "+v"(r[3]), [r4] "+v"(r[4]), [sv] "=&s"(sv), [t] "+v"(a0)
               : [a4] "v"(a4), [b] "s"(sb) : "vcc", "memory");
}
#define PL9(STEP) PL("dwordx4", "r0", "m0", "t") PADD(STEP) PL("dwordx4", "r1", "m1", "t") PADD(STEP) PL("dwordx4", "r2", "m2", "t") PADD(STEP) \
            PL("dwordx4", "r3", "m3", "t") PADD(STEP) PL("dwordx4", "r4", "m4", "t") PADD(STEP) PL("dwordx4", "r5", "m5", "t") PADD(STEP) \
            PL("dwordx4", "r6", "m6", "t") PADD(STEP) PL("dwordx4", "r7", "m7", "t") PADD(STEP) PL("dwordx4", "r8", "m8", "t")
__device__ __forceinline__ void poll_part(u32x4 (&r)[9], u32x2& hp, const void* sb, unsigned a0, unsigned a1) {
  u64 sv;
  asm volatile(PL("dwordx2", "hp", "mh", "a1") PL9("0x9000") PEND
               : PL7O, [r7] "+v"(r[7]), [r8] "+v"(r[8]), [hp] "+v"(hp)
               : PL7I, [m7] "v"(mx4(r[7])), [m8] "v"(mx4(r[8])), [mh] "v"(max(hp[0], hp[1])), [a1] "v"(a1) : "vcc", "memory");
}
__device__ __forceinline__ u32x4 gload16s_w(const void* sbase, unsigned voff) {
  u32x4 r; asm volatile("global_load_dwordx4 %0, %1, %2 sc1\n\ts_waitcnt vmcnt(0)" : "=&v"(r) : "v"(voff), "s"(sbase) : "memory"); return r;
}
__device__ __forceinline__ bf16x8 lds128(lds_cptr p) { return *(const LAS bf16x8*)p; }
__device__ __forceinline__ s16x4 ldstr(lds_cptr p) { return __builtin_amdgcn_ds_read_tr16_b64_v4i16((LAS s16x4*)p); }
__device__ __forceinline__ bf16x8 cat8(s16x4 a, s16x4 b) { return __builtin_shufflevector(a, b, 0, 1, 2, 3, 4, 5, 6, 7); }
__device__ __forceinline__ void st_u64(char* p, u64 v) { __hip_atomic_store((gu64*)p, v, __ATOMIC_RELAXED, __HIP_MEMORY_SCOPE_AGENT); }
__device__ __forceinline__ u64 ld_u64(const char* p) { return __hip_atomic_load((gu64*)p, __ATOMIC_RELAXED, __HIP_MEMORY_SCOPE_AGENT); }
constexpr unsigned SPIN_CAP = 1u << 18;
template <int CTRL> __device__ __forceinline__ unsigned dppu(unsigned x) { return (unsigned)__builtin_amdgcn_mov_dpp((int)x, CTRL, 0xf, 0xf, true); }
template <int CTRL> __device__ __forceinline__ float dppf(float x) { return __uint_as_float(dppu<CTRL>(__float_as_uint(x))); }
__device__ __forceinline__ float sum_x16(float x) { auto s = __builtin_amdgcn_permlane16_swap(__float_as_uint(x), __float_as_uint(x), false, false); return __uint_as_float(s[0]) + __uint_as_float(s[1]); }
__device__ __forceinline__ float sum_x32(float x) { auto s = __builtin_amdgcn_permlane32_swap(__float_as_uint(x), __float_as_uint(x), false, false); return __uint_as_float(s[0]) + __uint_as_float(s[1]); }
__device__ __forceinline__ float red32(float a, float b) { auto s = __builtin_amdgcn_permlane32_swap(__float_as_uint(a), __float_as_uint(b), false, false); return __uint_as_float(s[0]) + __uint_as_float(s[1]); }
__device__ __forceinline__ float red16(float a, float b) { auto s = __builtin_amdgcn_permlane16_swap(__float_as_uint(a), __float_as_uint(b), false, false); return __uint_as_float(s[0]) + __uint_as_float(s[1]); }
__device__ __forceinline__ float sum8(float x) { x += dppf<0xB1>(x); x += dppf<0x4E>(x); x += dppf<0x141>(x); return x; }
__device__ __forceinline__ float sum64(float x) { x = sum8(x); x += dppf<0x140>(x); return sum_x32(sum_x16(x)); }
__device__ __forceinline__ unsigned umax64(unsigned x) {
  x = max(x, dppu<0xB1>(x)); x = max(x, dppu<0x4E>(x)); x = max(x, dppu<0x141>(x)); x = max(x, dppu<0x140>(x));
  auto s = __builtin_amdgcn_permlane16_swap(x, x, false, false); x = max(s[0], s[1]);
  auto t = __builtin_amdgcn_permlane32_swap(x, x, false, false); return max(t[0], t[1]);
}
#define STAMP_DECL
#define STAMP(i)
#define CSTAMP(i)
#define STAMP_FLUSH(kid)

constexpr int P1_APITCH = 1168;
constexpr int P1_WBUF = 14336;
constexpr int P1_WOFF = 64 * P1_APITCH;
constexpr int LDS_BYTES = 163840;
static_assert(P1_WOFF + 6 * P1_WBUF <= LDS_BYTES, "P1 LDS");
__device__ __forceinline__ unsigned w_voff(int i, int j) {
  const int R = i / 27, cc = i - R * 27;
  return ((R & ~15) + 4 * (R & 3) + ((R >> 2) & 3)) * 3456 + j * 432 + cc * 16;
}
template <int N> __device__ __forceinline__ void w_dma(const char* wq, int c, int rot, const unsigned (&voff)[2], unsigned dst) {
  int kc = c + rot; kc = kc >= 18 ? kc - 18 : kc;
#pragma unroll
  for (int p = 0; p < N; ++p) glds16(wq + (size_t)kc * (32 * 3456), voff[p], dst + (c % 6) * P1_WBUF + p * 1024);
}
template <int V> __device__ __forceinline__ void wait_vm() { asm volatile("s_waitcnt vmcnt(%0)" :: "n"(V) : "memory"); }
template <int N, bool MM>
__device__ __forceinline__ void p1_main(f32x16 (&acc)[2], lds_cptr ap, lds_cptr bp, const char* wq, int rot, const unsigned (&voff)[2],
                                        unsigned dst, char* ws, unsigned soff) {
  bf16x8 wf[2], xa[2], xb[2];
  auto frags = [&](int c) {
    int kc = c + rot; kc = kc >= 18 ? kc - 18 : kc;
#pragma unroll
    for (int ks = 0; ks < 2; ++ks) {
      wf[ks] = cat8(ldstr(bp + (c % 6) * P1_WBUF + ks * 6912), ldstr(bp + (c % 6) * P1_WBUF + ks * 6912 + 432));
      xa[ks] = lds128(ap + kc * 64 + ks * 32); xb[ks] = lds128(ap + 32 * P1_APITCH + kc * 64 + ks * 32);
    }
  };
  wait_vm<5 * N>();
  LDS_BAR();
  if (MM) frags(0);
#pragma unroll
  for (int c = 0; c < 18; ++c) {
    if (N) { if (c == 0) wait_vm<4 * N>(); else if (c <= 13) wait_vm<3 * N>(); else if (c == 14) wait_vm<2 * N>(); else if (c == 15) wait_vm<N>(); else if (c == 16) wait_vm<0>(); }
    if (c < 17) asm volatile("s_barrier" ::: "memory");
    const bf16x8 w0 = wf[0], w1 = wf[1], a0 = xa[0], a1 = xa[1], b0 = xb[0], b1 = xb[1];
    int kn = c + 1 + rot; kn = kn >= 18 ? kn - 18 : kn;
    const lds_cptr bn = bp + ((c + 1) % 6) * P1_WBUF, an = ap + kn * 64;
    const bool rd = MM && c < 17;
    SB();
    if (MM) acc[0] = MFMA32(w0, a0, acc[0]);
    SB();
    if (rd) { wf[0] = cat8(ldstr(bn), ldstr(bn + 432)); xa[0] = lds128(an); xb[0] = lds128(an + 32 * P1_APITCH); }
    SB();
    if (MM) acc[1] = MFMA32(w0, b0, acc[1]);
    SB();
    if (rd) { wf[1] = cat8(ldstr(bn + 6912), ldstr(bn + 6912 + 432)); xa[1] = lds128(an + 32); xb[1] = lds128(an + 32 * P1_APITCH + 32); }
    SB();
    if (MM) acc[0] = MFMA32(w1, a1, acc[0]);
    SB();
    if (c >= 1 && c <= 12) w_dma<N>(wq, c + 5, rot, voff, dst);
    if (N == 0 && c < 8) { gstore16s(ws + c * 2048, soff, u32x4{SENT, SENT, SENT, SENT}); gstore16s(ws + c * 2048 + 1024, soff, u32x4{SENT, SENT, SENT, SENT}); }
    SB();
    if (MM) acc[1] = MFMA32(w1, b1, acc[1]);
    SB();
  }
}

struct AttnState { f32x16 sA, oA, oB; u32x4 pend; bf16x8 v1p, k1, k1n, v0, v1; s16x4 k2, k2n; };
struct QFrag { bf16x8 q1; s16x4 q2[4]; };
#define EXP2(x) __builtin_amdgcn_exp2f(x)
#define G4(P, i, S, r) do { P[i] = cvtpk(EXP2(S[r]), EXP2(S[(r) + 1])); P[(i) + 1] = cvtpk(EXP2(S[(r) + 2]), EXP2(S[(r) + 3])); } while (0)
__device__ __forceinline__ s16x4 ldk2(lds_cptr p) { return __builtin_bit_cast(s16x4, u32x2{*(const LAS unsigned*)p, *(const LAS unsigned*)(p + 128)}); }
__device__ __forceinline__ bf16x8 ldv(lds_cptr p) { return cat8(ldstr(p), ldstr(p + 320)); }
__device__ __forceinline__ void attn_prime(AttnState& st, lds_cptr kp, lds_cptr k2p, lds_cptr vp, const QFrag& qa) {
  st.k1 = lds128(kp); st.k2 = ldk2(k2p); st.k1n = lds128(kp + 1024); st.v0 = ldv(vp); st.v1 = ldv(vp + 640);
  st.sA = MFMA32K8(st.k2, qa.q2[0], MFMA32(st.k1, qa.q1, f32x16{}));
}
template <int PR, class HOOK>
__device__ __forceinline__ void attn8(AttnState& st, lds_cptr kp, lds_cptr k2p, lds_cptr vp, int nx, const QFrag& qa, const QFrag& qb, HOOK hook) {
#pragma unroll
  for (int t = 0; t < 8; ++t) {
    u32x4 p0, p1; f32x16 sB;
    const int pr = PR == 0 ? ((t & 1) ? -1 : 3 - (t >> 1)) : (t & 3) ? -1 : (PR == 1 ? 3 : 1) - (t >> 2);
    if (pr == 0) __builtin_amdgcn_s_setprio(0);
    if (pr == 1) __builtin_amdgcn_s_setprio(1);
    if (pr == 2) __builtin_amdgcn_s_setprio(2);
    if (pr == 3) __builtin_amdgcn_s_setprio(3);
    __builtin_amdgcn_s_waitcnt(0xC07F);
    const bf16x8 v0n = ldv(t < 7 ? vp + (t + 1) * 1280 : vp + nx), v1n = ldv(t < 7 ? vp + (t + 1) * 1280 + 640 : vp + nx + 640);
    const bf16x8 k1nn = lds128(t < 6 ? kp + (t + 2) * 1024 : kp + nx + (t - 6) * 1024);
    if (t == 2) st.k2n = ldk2(k2p + 512);
    if (t == 6) st.k2n = ldk2(k2p + nx);
    hook(t);
    SB();
    sB = MFMA32(st.k1, qb.q1, f32x16{}); SB();
    G4(p0, 0, st.sA, 0); SB();
    st.oB = MFMA32(st.v1p, __builtin_bit_cast(bf16x8, st.pend), st.oB); SB();
    G4(p0, 2, st.sA, 4); SB();
    sB = MFMA32K8(st.k2, qb.q2[t & 3], sB); SB();
    G4(p1, 0, st.sA, 8); SB();
    st.oA = MFMA32(st.v0, __builtin_bit_cast(bf16x8, p0), st.oA); SB();
    G4(p1, 2, st.sA, 12); SB();
    st.sA = MFMA32(st.k1n, qa.q1, f32x16{}); SB();
    G4(p0, 0, sB, 0); SB();
    st.oA = MFMA32(st.v1, __builtin_bit_cast(bf16x8, p1), st.oA); SB();
    G4(p0, 2, sB, 4); SB();
    if ((t & 3) == 3) st.k2 = st.k2n;
    st.sA = MFMA32K8(st.k2, qa.q2[(t + 1) & 3], st.sA); SB();
    G4(p1, 0, sB, 8); SB();
    st.oB = MFMA32(st.v0, __builtin_bit_cast(bf16x8, p0), st.oB); SB();
    G4(p1, 2, sB, 12); SB();
    st.pend = p1; st.v1p = st.v1; st.k1 = st.k1n; st.k1n = k1nn; st.v0 = v0n; st.v1 = v1n;
  }
  PIN(st.oA); PIN(st.oB); PIN(st.pend); PIN(st.sA);
}
__device__ __forceinline__ int kv_goff(int u) {
  const int slot = u / 19, pc = u - slot * 19;
  return ((slot & 1) * 4 + (slot >> 1)) * CHUNK_B + pc * 1024;
}
constexpr int P2_XOFF = 82944;
constexpr int P3_XOFF = 116736;
constexpr int FB_OFF = 137216;
constexpr int P3_SOFF = 147456;

__global__ void __launch_bounds__(512, 2) k_block(const char* __restrict__ x, const char* __restrict__ wqkv, const char* __restrict__ wo,
                                                  const char* __restrict__ wr, const char* __restrict__ wg, const char* __restrict__ wu,
                                                  const char* __restrict__ wd, char* __restrict__ out, char* __restrict__ ws) {
  extern __shared__ __attribute__((aligned(16))) char smem[];
  const int tid = threadIdx.x, lane = tid & 63, w = __builtin_amdgcn_readfirstlane(tid >> 6);
  const int b = blockIdx.x, xc = b & 7, sl = b >> 3;
  const unsigned lds0 = (unsigned)(uintptr_t)smem;
  const int h = lane >> 5, r32 = lane & 31;
  STAMP_DECL STAMP(0);
  const char* pc0_ = (const char*)__builtin_amdgcn_s_getpc();
  u64 tag;
  {
    const int j = 4 * (xc >> 2) + (sl >> 3), rb = 8 * (xc & 3) + (sl & 7);
    const int row = tid >> 3, j8 = tid & 7;
    u32x4 xr[9];
    {
      const char* xb = x + (size_t)rb * 64 * 1152;
      const unsigned vo = row * 1152 + j8 * 16;
#pragma unroll
      for (int i = 0; i < 9; ++i) xr[i] = gload16(xb + i * 128, vo);
    }
    u32x2 ep = gload8s(ws, WS_EPOCH);
    u32x4 cpf = u32x4{0, 0, 0, 0};
    if (w == 0) cpf = gload16(pc0_, (sl + 32 * (lane >> 5)) * 768 + r32 * 24);
    const int rot = ((sl & 7) * 9) >> 2;
    const int pc0 = 2 * max(w - 1, 0);
    unsigned voff[2];
#pragma unroll
    for (int p = 0; p < 2; ++p) voff[p] = w_voff(min((pc0 + p) * 64 + lane, 863), j);
    const unsigned wdst = lds0 + P1_WOFF + pc0 * 1024;
    asm volatile("s_barrier" ::: "memory");
    if (w) {
#pragma unroll
      for (int c = 0; c < 3; ++c) w_dma<2>(wqkv, c, rot, voff, wdst);
      WAIT_VM(6);
    } else {
      WAIT_VM(0);
    }
#pragma unroll
    for (int i = 0; i < 9; ++i) PIN(xr[i]);
    PIN(ep); PIN(cpf);
    tag = (((u64)ep[1] << 32) | ep[0]) * 6364136223846793005ull + 1442695040888963407ull;
    STAMP(1);
    {
      float s = 0.f, q = 0.f;
#pragma unroll
      for (int i = 0; i < 9; ++i)
#pragma unroll
        for (int e = 0; e < 4; ++e) { s = dot2(xr[i][e], 0x3F803F80u, s); q = dot2(xr[i][e], xr[i][e], q); }
      if (w) {
#pragma unroll
        for (int c = 3; c < 6; ++c) w_dma<2>(wqkv, c, rot, voff, wdst);
      }
      s = sum8(s); q = sum8(q);
      const float m = s * (1.f / 576.f);
      const float var = fmaxf(fmaf(q, 1.f / 576.f, -m * m), 0.f);
      const float rinv = 1.f / rbf(sqrtf(rbf(rbf(var) + 1e-5f)));
      const float nb = -rbf(m) * rinv;
      const lds_ptr ap = (lds_ptr)smem + row * P1_APITCH + j8 * 16;
#pragma unroll
      for (int i = 0; i < 9; ++i) {
        u32x4 o;
#pragma unroll
        for (int e = 0; e < 4; ++e) o[e] = cvtpk(fmaf(bflo(xr[i][e]), rinv, nb), fmaf(bfhi(xr[i][e]), rinv, nb));
        *(LAS u32x4*)(ap + i * 128) = o;
      }
    }
    STAMP(2); CSTAMP(2);
    f32x16 acc[2]; acc[0] = f32x16{}; acc[1] = f32x16{};
    {
      const int sg = 4 * ((lane >> 4) & 1) + (lane & 3);
      const int col = w < 6 ? 36 * w + 4 * ((0x86753120u >> (4 * sg)) & 15) : 36 * min(sg, 5) + 16;
      const lds_cptr ap = (lds_cptr)smem + r32 * P1_APITCH + h * 16;
      const lds_cptr bp = (lds_cptr)smem + P1_WOFF + (4 * ((lane & 15) >> 2) + 2 * h) * 432 + col * 2;
      const unsigned soff = WS_SENT + b * 16384 + lane * 16;
      if (w == 7) p1_main<2, false>(acc, ap, bp, wqkv, rot, voff, wdst, ws, soff);
      else if (w == 0) p1_main<0, true>(acc, ap, bp, wqkv, rot, voff, wdst, ws, soff);
      else p1_main<2, true>(acc, ap, bp, wqkv, rot, voff, wdst, ws, soff);
    }
    STAMP(3);
    constexpr float QS = 0.23570226039551584f * 1.4426950408889634f;
    const int t0 = 2 * rb;
    const int kvt = WS_KV + (t0 >> 3) * CHUNK_B;
    const lds_ptr vst = (lds_ptr)smem + P1_WOFF + (r32 >> 2) * 160;
    if (w < 6) {
      const int P = 6 * j + w, ty = P >> 4, hd = 2 * (P & 15);
      const float sc = ty ? 1.f : QS;
      const int base = (ty ? kvt + hd * HEAD_B + (t0 & 7) * 1024 : WS_Q1 + hd * 65536 + t0 * 1024) + lane * 16;
      const int hs = ty ? HEAD_B : 65536;
#pragma unroll
      for (int mi = 0; mi < 2; ++mi)
#pragma unroll
        for (int ab = 0; ab < 2; ++ab) {
          u32x4 o;
#pragma unroll
          for (int e = 0; e < 4; ++e) o[e] = cvtpk(acc[mi][8 * ab + 2 * e] * sc, acc[mi][8 * ab + 2 * e + 1] * sc);
          if (ty == 2) *(LAS u32x4*)(vst + (2 * w + ab) * 2560 + mi * 1280 + (r32 & 3) * 32 + h * 16) = o;
          else gstore16s(ws, base + ab * hs + mi * 1024, o);
        }
    } else if (w == 6) {
#pragma unroll
      for (int rq = 0; rq < 3; ++rq) {
        const int P = 6 * j + 2 * rq, ty = P >> 4, hd = 2 * ((P + h) & 15);
        const float sc = ty ? 1.f : QS;
#pragma unroll
        for (int mi = 0; mi < 2; ++mi) {
          const unsigned oa = cvtpk(acc[mi][4 * rq] * sc, acc[mi][4 * rq + 1] * sc), ob = cvtpk(acc[mi][4 * rq + 2] * sc, acc[mi][4 * rq + 3] * sc);
          if (ty == 2) {
            const lds_ptr d = vst + (4 * rq + 2 * h) * 2560 + mi * 1280 + 128 + (r32 & 3) * 8;
            *(LAS u32x2*)d = u32x2{oa, 0x00003F80u}; *(LAS u32x2*)(d + 2560) = u32x2{ob, 0x00003F80u};
          } else {
            const int base = (ty ? kvt + hd * HEAD_B + 8192 + (t0 & 7) * 128 : WS_Q2 + hd * 8192 + t0 * 128) + mi * 128 + r32 * 4;
            gstore4s(ws, base, oa); gstore4s(ws, base + (ty ? HEAD_B : 8192), ob);
          }
        }
      }
    }
    if (j >= 5) {
      LDS_BAR();
#pragma unroll
      for (int i = 0; i < 4; ++i) {
        const int pi = i * 512 + tid + (j == 5 ? 640 : 0), hh = pi / 160;
        if (pi < 1920) gstore16s(ws, kvt + (12 * j + hh - 64) * HEAD_B + 9216 + (t0 & 7) * 1280 + (pi - hh * 160) * 16,
                                 *(const LAS u32x4*)(smem + P1_WOFF + pi * 16));
      }
    }
    WAIT_BAR(0);
    if (tid == 0) st_u64(ws + WS_FLAG1 + (j * 32 + rb) * 8, tag);
    STAMP(4); CSTAMP(3);
  }
  const int panel = 4 * xc + (sl & 3), cg = sl >> 2;
  const int mt = w & 3, kq = w >> 2, g = lane >> 4, l16 = lane & 15;
  const int nnt = kq ? 2 : 3, nt0 = kq ? 3 : 0;
  const int tok = panel * 64 + mt * 16 + l16;
  const bool ffn = b < 144;
  const int fs = ((b & 7) + 8 * (b >> 4)) * 2 + ((b >> 3) & 1);
  u32x2 xres[3], wrr[3], gw[9], uw[9];
  unsigned wrow[5];
  u32x4 dw[8];
  {
    const int head = 4 * xc + (sl & 3), qp = sl >> 2;
    const int qpair = w & 3, kh = w >> 2;
    if (w == 0) {
      const char* f1 = ws + WS_FLAG1 + ((h ? (64 + head) / 12 : (32 + head) / 12) * 32 + r32) * 8;
      const char* f2 = ws + WS_FLAG1 + ((head / 12) * 32 + 4 * qp + (lane & 3)) * 8;
      _Pragma("unroll 1") for (unsigned spin = 0; spin < SPIN_CAP; ++spin) {
        const u64 a = ld_u64(f1), c = ld_u64(f2);
        if (__all(a == tag && c == tag)) break;
        __builtin_amdgcn_s_sleep(1);
      }
    }
    LDS_BAR();
    STAMP(5);
    const char* img = ws + WS_KV + (size_t)head * HEAD_B;
    const int qt = qp * 8 + 2 * qpair;
    u32x4 kv[19];
#pragma unroll
    for (int i = 0; i < 5; ++i) kv[i] = gload16s(img + kv_goff(8 * i + w), lane * 16);
    u32x4 q1a = gload16s(ws, WS_Q1 + head * 65536 + qt * 1024 + lane * 16);
    u32x4 q1b = gload16s(ws, WS_Q1 + head * 65536 + qt * 1024 + 1024 + lane * 16);
    unsigned q2a = gload4s(ws, WS_Q2 + head * 8192 + qt * 128 + r32 * 4);
    unsigned q2b = gload4s(ws, WS_Q2 + head * 8192 + qt * 128 + 128 + r32 * 4);
#pragma unroll
    for (int i = 5; i < 8; ++i) kv[i] = gload16s(img + kv_goff(8 * i + w), lane * 16);
    const lds_ptr kdst = (lds_ptr)smem + w * 1024 + lane * 16;
    WAIT_VM(3);
#pragma unroll
    for (int i = 0; i < 5; ++i) { PIN(kv[i]); *(LAS u32x4*)(kdst + i * 8192) = kv[i]; }
    PIN(q1a); PIN(q1b); PIN(q2a); PIN(q2b);
    QFrag qa, qb;
    qa.q1 = __builtin_bit_cast(bf16x8, q1a); qb.q1 = __builtin_bit_cast(bf16x8, q1b);
#pragma unroll
    for (int i = 0; i < 4; ++i) {
      const unsigned ua = h == (i >> 1) ? q2a : 0u, ub = h == (i >> 1) ? q2b : 0u;
      qa.q2[i] = __builtin_bit_cast(s16x4, (i & 1) ? u32x2{0u, ua} : u32x2{ua, 0u});
      qb.q2[i] = __builtin_bit_cast(s16x4, (i & 1) ? u32x2{0u, ub} : u32x2{ub, 0u});
    }
    const int vlo = h * 160 + ((lane & 16) ? 128 + (l16 >> 2) * 8 : l16 * 8);
    const lds_cptr kp = (lds_cptr)smem + kh * CHUNK_B + lane * 16;
    const lds_cptr k2p = (lds_cptr)smem + kh * CHUNK_B + 8192 + h * 256 + r32 * 4;
    const lds_cptr vp = (lds_cptr)smem + kh * CHUNK_B + 9216 + vlo;
    LDS_BAR();
    STAMP(6); CSTAMP(0);
    AttnState st;
    st.oA = f32x16{}; st.oB = f32x16{}; st.pend = u32x4{0, 0, 0, 0}; st.v1p = bf16x8{};
    attn_prime(st, kp, k2p, vp, qa);
    attn8<0>(st, kp, k2p, vp, 0, qa, qb, [&](int t) {
#pragma unroll
      for (int i = 8 + 3 * t; i < 11 + 3 * t; ++i) if (i < 19) kv[i] = gload16s(img + kv_goff(8 * i + w), lane * 16);
    });
    STAMP(14);
    WAIT_VM(0);
#pragma unroll
    for (int i = 5; i < 19; ++i) { PIN(kv[i]); *(LAS u32x4*)(kdst + i * 8192) = kv[i]; }
    LDS_BAR();
    STAMP(15);
    attn_prime(st, kp + 2 * CHUNK_B, k2p + 2 * CHUNK_B, vp + 2 * CHUNK_B, qa);
    const int ycol = l16 < 8 ? w * 64 + l16 * 8 : 512 + w * 8;
    const unsigned dn = ffn ? (fs * 32 + g * 8) * 1152 + ycol * 2 : 0u;
    const unsigned go = ffn ? (tid >> 3) * (F_ * 2) + fs * 64 + (tid & 7) * 8 : 0u;
    attn8<1>(st, kp + 2 * CHUNK_B, k2p + 2 * CHUNK_B, vp + 2 * CHUNK_B, 2 * CHUNK_B, qa, qb, [&](int t) {
      gw[t] = gload8(wg + t * 64 * (F_ * 2), go);
      if (t == 7) gw[8] = gload8(wg + 8 * 64 * (F_ * 2), go);
      if (t == 1 || t == 3 || t == 5) dw[t >> 1] = gload16(wd + (t >> 1) * 1152, dn);
    });
    STAMP(16);
    attn8<2>(st, kp + 4 * CHUNK_B, k2p + 4 * CHUNK_B, vp + 4 * CHUNK_B, 2 * CHUNK_B, qa, qb, [&](int t) {
      uw[t] = gload8(wu + t * 64 * (F_ * 2), go);
      if (t == 7) uw[8] = gload8(wu + 8 * 64 * (F_ * 2), go);
      if (t == 1 || t == 3 || t == 5) dw[3 + (t >> 1)] = gload16(wd + (3 + (t >> 1)) * 1152, dn);
      if (t < 3) {
        const int n = min((nt0 + t) * 16 + 4 * g, 68);
        xres[t] = gload8(x, tok * 1152 + (cg * 72 + n) * 2);
        wrr[t] = gload8(wr, (cg * 72 + n) * 2);
      } else {
        wrow[t - 3] = gload4(wr, min((t - 3) * 64 + lane, 287) * 4);
      }
    });
    STAMP(17);
    u32x4 fa, fb;
    LDS_BAR();
    attn8<0>(st, kp + 6 * CHUNK_B, k2p + 6 * CHUNK_B, vp + 6 * CHUNK_B, 0, qa, qb, [&](int t) {
#pragma unroll
      for (int i = 3 * t; i < 3 * t + 3; ++i) {
        if (i < 11) {
          const int pc = i < 10 ? 8 * i + w : 80;
          const int ci = pc * 64 + lane;
          const int R = ci / 9, cc = ci - R * 9;
          glds16(wo, R * 1152 + cg * 144 + cc * 16, lds0 + pc * 1024);
        }
      }
      if (t < 2) dw[6 + t] = gload16(wd + (6 + t) * 1152, dn);
      if (t == 4) { fa = gload16s(ws, WS_FLAG1 + lane * 32); fb = gload16s(ws, WS_FLAG1 + lane * 32 + 16); }
    });
    st.oB = MFMA32(st.v1p, __builtin_bit_cast(bf16x8, st.pend), st.oB);
    STAMP(7); CSTAMP(1);
    LAS f32x4* xw = (LAS f32x4*)(smem + P2_XOFF) + (qpair * 2 + kh) * 192 + lane;
    LAS f32x4* xr_ = (LAS f32x4*)(smem + P2_XOFF) + (qpair * 2 + (kh ^ 1)) * 192 + lane;
    f32x16 o = kh ? st.oB : st.oA;
    {
      const f32x16 give = kh ? st.oA : st.oB;
#pragma unroll
      for (int r = 0; r < 3; ++r) xw[r * 64] = f32x4{give[4 * r], give[4 * r + 1], give[4 * r + 2], give[4 * r + 3]};
    }
    WAIT_BAR(0);
#pragma unroll
    for (int r = 0; r < 3; ++r) {
      const f32x4 t = xr_[r * 64];
#pragma unroll
      for (int e = 0; e < 4; ++e) o[4 * r + e] += t[e];
    }
    PIN(fa); PIN(fb);
    _Pragma("unroll 1") for (unsigned spin = 0; spin < SPIN_CAP; ++spin) {
      const unsigned t0 = (unsigned)tag, t1 = (unsigned)(tag >> 32);
      if (__all(fa[0] == t0 && fa[1] == t1 && fa[2] == t0 && fa[3] == t1 && fb[0] == t0 && fb[1] == t1 && fb[2] == t0 && fb[3] == t1)) break;
      fa = gload16s_w(ws, WS_FLAG1 + lane * 32); fb = gload16s_w(ws, WS_FLAG1 + lane * 32 + 16);
    }
    float l;
    {
      auto sw = __builtin_amdgcn_permlane32_swap(__float_as_uint(o[10]), __float_as_uint(o[10]), false, false);
      l = __uint_as_float(sw[0]);
    }
    bool ok = (l > 8.6736174e-19f) && (l < 1.1529215e18f);
#pragma unroll
    for (int r = 0; r < 10; ++r) ok = ok && ((h && r >= 8) || fabsf(o[r]) < 3.0e38f);
    if (!__all(ok)) {
      const bf16x8 qm1 = kh ? qb.q1 : qa.q1; const s16x4 qm2 = kh ? qb.q2[0] : qa.q2[0];
      const lds_ptr fb = (lds_ptr)smem + FB_OFF + w * 1280;
      float mx = 0.f;
#pragma unroll 1
      for (int pass = 0; pass < 2; ++pass) {
        f32x16 cin;
#pragma unroll
        for (int r = 0; r < 16; ++r) cin[r] = -mx;
        float m2 = -INFINITY;
        o = f32x16{};
#pragma unroll 1
        for (int kt = 0; kt < 64; ++kt) {
          const char* cb = img + (kt >> 3) * CHUNK_B;
          u32x4 k1 = gload16s(cb + (kt & 7) * 1024, lane * 16);
          unsigned k2 = gload4s(cb + 8192 + (kt & 7) * 128, r32 * 4);
          u32x4 va = gload16s(cb + 9216 + (kt & 7) * 1280, lane * 16), vb = gload16s(cb + 9216 + (kt & 7) * 1280 + 1024, l16 * 16);
          WAIT_VM(0); PIN(k1); PIN(k2); PIN(va); PIN(vb);
          *(LAS u32x4*)(fb + lane * 16) = va; *(LAS u32x4*)(fb + 1024 + l16 * 16) = vb;
          f32x16 s = MFMA32(__builtin_bit_cast(bf16x8, k1), qm1, cin);
          s = MFMA32K8(__builtin_bit_cast(s16x4, (u32x2{k2, 0u})), qm2, s);
#pragma unroll
          for (int r = 0; r < 16; ++r) m2 = fmaxf(m2, s[r]);
          u32x4 p0, p1;
          G4(p0, 0, s, 0); G4(p0, 2, s, 4); G4(p1, 0, s, 8); G4(p1, 2, s, 12);
          asm volatile("s_waitcnt lgkmcnt(0)" ::: "memory");
          o = MFMA32(ldv((lds_cptr)fb + vlo), __builtin_bit_cast(bf16x8, p0), o);
          o = MFMA32(ldv((lds_cptr)fb + vlo + 640), __builtin_bit_cast(bf16x8, p1), o);
          asm volatile("s_waitcnt lgkmcnt(0)" ::: "memory");
        }
        auto sw = __builtin_amdgcn_permlane32_swap(__float_as_uint(m2), __float_as_uint(m2), false, false);
        mx = fmaxf(__uint_as_float(sw[0]), __uint_as_float(sw[1]));
      }
      auto sw = __builtin_amdgcn_permlane32_swap(__float_as_uint(o[10]), __float_as_uint(o[10]), false, false);
      l = __uint_as_float(sw[0]);
    }
    const float rl = 1.f / l;
    u32x4 ov;
#pragma unroll
    for (int e = 0; e < 4; ++e) ov[e] = nsent(cvtpk(o[2 * e] * rl, o[2 * e + 1] * rl));
    gstore16s(ws, WS_O1 + head * 65536 + (qt + kh) * 1024 + lane * 16, ov);
    if (!h) gstore4s(ws, WS_O2 + head * 8192 + ((qt + kh) * 32 + r32) * 4, nsent(cvtpk(o[8] * rl, o[9] * rl)));
    STAMP(8);
  }
  float wsum;
  {
#pragma unroll
    for (int i = 0; i < 5; ++i) PIN(wrow[i]);
    if (h) wrow[4] = 0u;
    float t = 0.f;
#pragma unroll
    for (int i = 0; i < 5; ++i) t = dot2(wrow[i], 0x3F803F80u, t);
    wsum = sum64(t);
  }
  f32x4 acc[5];
  {
    u32x4 of[9];
    const unsigned o1 = WS_O1 + (kq * 18 + (g >> 1)) * 65536 + (2 * panel + (mt >> 1)) * 1024 + (g & 1) * 512 + ((mt & 1) * 16 + l16) * 16;
    const int q = l16 >> 2;
    const lds_cptr bp = (lds_cptr)smem + (kq * 9 * 36 + 20 * (g >> 1) + 4 * (g & 1) + q) * 144 + (lane & 3) * 8;
    const lds_cptr bp2 = (lds_cptr)smem + (18 * (4 * g + (q >> 1)) + ((q >> 1) ? 0 : 16) + (q & 1)) * 144 + (lane & 3) * 8;
    bf16x8 wpf[25];
#pragma unroll
    for (int i = 0; i < 25; ++i) {
      wpf[i] = cat8(ldstr(bp + (i / 5) * (36 * 144) + (i % 5) * 32), ldstr(bp + (i / 5) * (36 * 144) + 8 * 144 + (i % 5) * 32));
      PIN(wpf[i]);
    }
#pragma unroll
    for (int i = 0; i < 9; ++i) of[i] = u32x4{SENT, SENT, SENT, SENT};
    if (kq == 0) {
      _Pragma("unroll 1") for (unsigned spin = 0; spin < SPIN_CAP; ++spin) {
        poll_o9(of, ws, o1);
        unsigned m = 0;
#pragma unroll
        for (int i = 0; i < 9; ++i) m = max(m, mx4(of[i]));
        if (__all(m != SENT)) break;
        __builtin_amdgcn_s_sleep(1);
      }
    } else {
      unsigned d[8] = {SENT, SENT, SENT, SENT, SENT, SENT, SENT, SENT};
      _Pragma("unroll 1") for (unsigned spin = 0; spin < SPIN_CAP; ++spin) {
        poll_o7(of, d, ws, o1, WS_O2 + g * 32768 + tok * 4);
        of[7] = u32x4{d[0], d[1], d[2], d[3]}; of[8] = u32x4{d[4], d[5], d[6], d[7]};
        unsigned m = 0;
#pragma unroll
        for (int i = 0; i < 9; ++i) m = max(m, mx4(of[i]));
        if (__all(m != SENT)) break;
        __builtin_amdgcn_s_sleep(1);
      }
    }
    STAMP(9);
#pragma unroll
    for (int i = 0; i < 5; ++i) acc[i] = f32x4{};
#pragma unroll
    for (int ks = 0; ks < 9; ++ks) {
      const bool sp = ks >= 7 && kq;
      const lds_cptr b0 = ks >= 7 ? (sp ? bp2 + (ks - 7) * (288 * 144) : bp + ks * (36 * 144)) : bp + ks * (36 * 144);
      const int d2 = sp ? 36 * 144 : 8 * 144;
#pragma unroll
      for (int i = 0; i < 5; ++i) {
        const bf16x8 wf = ks < 5 ? wpf[ks * 5 + i] : cat8(ldstr(b0 + i * 32), ldstr(b0 + d2 + i * 32));
        acc[i] = MFMA16(wf, __builtin_bit_cast(bf16x8, of[ks]), acc[i]);
      }
    }
    LAS f32x4* xw = (LAS f32x4*)(smem + P3_XOFF) + mt * 320 + lane;
    if (kq == 0) { xw[3 * 64] = acc[3]; xw[4 * 64] = acc[4]; } else { xw[0] = acc[0]; xw[64] = acc[1]; xw[128] = acc[2]; }
    LDS_BAR();
    if (kq == 0) { acc[0] += xw[0]; acc[1] += xw[64]; acc[2] += xw[128]; }
    else { acc[0] = acc[3] + xw[3 * 64]; acc[1] = acc[4] + xw[4 * 64]; acc[2] = f32x4{}; }
  }
#pragma unroll
  for (int i = 0; i < 3; ++i) { PIN(xres[i]); PIN(wrr[i]); }
  STAMP(10);
  const int rgp = panel * 4 + mt;
  float hv[3][4];
  unsigned key; float mb, rinv;
  {
    float s1 = 0.f, s2 = 0.f, s3 = 0.f;
#pragma unroll
    for (int i = 0; i < 3; ++i) {
      const bool valid = (i < nnt) && ((nt0 + i) * 16 + 4 * g < 72);
      const unsigned a01 = cvtpk(acc[i][0], acc[i][1]), a23 = cvtpk(acc[i][2], acc[i][3]);
      unsigned h01 = cvtpk(bflo(xres[i][0]) + bflo(a01), bfhi(xres[i][0]) + bfhi(a01));
      unsigned h23 = cvtpk(bflo(xres[i][1]) + bflo(a23), bfhi(xres[i][1]) + bfhi(a23));
      h01 = valid ? h01 : 0u; h23 = valid ? h23 : 0u;
      hv[i][0] = bflo(h01); hv[i][1] = bfhi(h01); hv[i][2] = bflo(h23); hv[i][3] = bfhi(h23);
      s1 += (hv[i][0] + hv[i][1]) + (hv[i][2] + hv[i][3]);
      s2 = fmaf(hv[i][0], hv[i][0], fmaf(hv[i][1], hv[i][1], fmaf(hv[i][2], hv[i][2], fmaf(hv[i][3], hv[i][3], s2))));
      s3 = fmaf(hv[i][0], bflo(wrr[i][0]), fmaf(hv[i][1], bfhi(wrr[i][0]), fmaf(hv[i][2], bflo(wrr[i][1]), fmaf(hv[i][3], bfhi(wrr[i][1]), s3))));
    }
    s1 = sum_x32(sum_x16(s1)); s2 = sum_x32(sum_x16(s2)); s3 = sum_x32(sum_x16(s3));
    if (g == 0) gstore16s(ws, SY_STATS + rgp * 4096 + (cg * 2 + kq) * 256 + l16 * 16,
                          u32x4{nsent(__float_as_uint(s1)), nsent(__float_as_uint(s2)), nsent(__float_as_uint(s3)), 0u});
    u32x4 sv[4];
#pragma unroll
    for (int i = 0; i < 4; ++i) sv[i] = u32x4{SENT, SENT, SENT, SENT};
    _Pragma("unroll 1") for (unsigned spin = 0; spin < SPIN_CAP; ++spin) {
      poll_4x16(sv, ws, SY_STATS + rgp * 4096 + lane * 16);
      if (__all(max(max(mx4(sv[0]), mx4(sv[1])), max(mx4(sv[2]), mx4(sv[3]))) != SENT)) break;
      __builtin_amdgcn_s_sleep(1);
    }
    STAMP(11);
#define SUMC(c) sum_x32(sum_x16((__uint_as_float(sv[0][c]) + __uint_as_float(sv[1][c])) + (__uint_as_float(sv[2][c]) + __uint_as_float(sv[3][c]))))
    s1 = SUMC(0); s2 = SUMC(1); s3 = SUMC(2);
    const float m = s1 * (1.f / 576.f);
    mb = rbf(m);
    rinv = 1.f / rbf(sqrtf(rbf(rbf(fmaxf(fmaf(s2, 1.f / 576.f, -m * m), 0.f)) + 1e-5f)));
    const unsigned u = __float_as_uint((s3 - mb * wsum) * rinv);
    const unsigned ord = (u & 0x80000000u) ? ~u : (u | 0x80000000u);
    key = (min(ord, 0xFFFFF000u) & ~0x7FFu) | (unsigned)(2047 - tok);
    key = max(key, dppu<0xB1>(key)); key = max(key, dppu<0x4E>(key)); key = max(key, dppu<0x141>(key)); key = max(key, dppu<0x140>(key));
  }
  if (cg == 0 && kq == 0 && lane < 16) gstore4s(ws, SY_BEST + lane * 4096 + rgp * 4, key);
  const bool isbest = tok == 2047 - (int)(key & 0x7FFu);
  u32x2 ov[3];
#pragma unroll
  for (int i = 0; i < 3; ++i) {
    const bool valid = (i < nnt) && ((nt0 + i) * 16 + 4 * g < 72);
    const unsigned d01 = cvtpk(hv[i][0] - mb, hv[i][1] - mb), d23 = cvtpk(hv[i][2] - mb, hv[i][3] - mb);
    const unsigned n01 = cvtpk(bflo(d01) * rinv, bfhi(d01) * rinv), n23 = cvtpk(bflo(d23) * rinv, bfhi(d23) * rinv);
    ov[i] = u32x2{cvtpk(hv[i][0] + bflo(n01), hv[i][1] + bfhi(n01)), cvtpk(hv[i][2] + bflo(n23), hv[i][3] + bfhi(n23))};
    const int col = cg * 72 + (nt0 + i) * 16 + 4 * g;
    if (valid && isbest) {
      gstore8s(ws, SY_CHN + rgp * 1152 + col * 2, u32x2{nsent(n01), nsent(n23)});
      gstore8s(ws, SY_CH + rgp * 1152 + col * 2, u32x2{nsent(cvtpk(hv[i][0], hv[i][1])), nsent(cvtpk(hv[i][2], hv[i][3]))});
    }
  }
  if (w != 0 && !isbest) {
#pragma unroll
    for (int i = 0; i < 3; ++i) {
      const bool valid = (i < nnt) && ((nt0 + i) * 16 + 4 * g < 72);
      if (valid) *(u32x2*)(out + (size_t)tok * 1152 + (cg * 72 + (nt0 + i) * 16 + 4 * g) * 2) = ov[i];
    }
  }
  float gf[9][8], df[8][8];
#pragma unroll
  for (int i = 0; i < 9; ++i) {
    PIN(gw[i]); PIN(uw[i]);
    gf[i][0] = bflo(gw[i][0]); gf[i][1] = bfhi(gw[i][0]); gf[i][2] = bflo(gw[i][1]); gf[i][3] = bfhi(gw[i][1]);
    gf[i][4] = bflo(uw[i][0]); gf[i][5] = bfhi(uw[i][0]); gf[i][6] = bflo(uw[i][1]); gf[i][7] = bfhi(uw[i][1]);
  }
#pragma unroll
  for (int i = 0; i < 8; ++i) {
    PIN(dw[i]);
#pragma unroll
    for (int e = 0; e < 4; ++e) { df[i][2 * e] = bflo(dw[i][e]); df[i][2 * e + 1] = bfhi(dw[i][e]); }
  }
#pragma unroll
  for (int i = 0; i < 72; ++i) PIN(gf[i >> 3][i & 7]);
#pragma unroll
  for (int i = 0; i < 64; ++i) PIN(df[i >> 3][i & 7]);
  LAS unsigned* xs = (LAS unsigned*)(smem + P3_SOFF + 8192);
  if (w == 0) {
    u32x2 a = u32x2{SENT, SENT};
    _Pragma("unroll 1") for (unsigned spin = 0; spin < SPIN_CAP; ++spin) {
      poll_8(a, ws, SY_BEST + (b & 15) * 4096 + lane * 8);
      if (__all(max(a[0], a[1]) != SENT)) break;
      __builtin_amdgcn_s_sleep(1);
    }
    const int s0 = __builtin_amdgcn_readfirstlane(2047 - (int)(umax64(max(a[0], a[1])) & 0x7FFu));
    STAMP(12);
    if (b < 180) {
      unsigned xv[5] = {SENT, SENT, SENT, SENT, SENT};
      _Pragma("unroll 1") for (unsigned spin = 0; spin < SPIN_CAP; ++spin) {
        poll_5x4(xv, ws, SY_CHN + (s0 >> 4) * 1152 + lane * 4, SY_CHN + (s0 >> 4) * 1152 + 1024 + r32 * 4);
        unsigned m = 0;
#pragma unroll
        for (int i = 0; i < 5; ++i) m = max(m, xv[i]);
        if (__all(m != SENT)) break;
      }
#pragma unroll
      for (int i = 0; i < 5; ++i) { xs[i * 64 + lane] = xv[i]; *(LAS u32x2*)(xs + 384 + i * 128 + lane * 2) = u32x2{xv[i] << 16, xv[i] & 0xffff0000u}; }
    }
    xs[320] = s0;
  }
  LDS_BAR();
  const int sel = __builtin_amdgcn_readfirstlane(xs[320]);
  const int wgrp = sel >> 4;
  if (ffn) {
    STAMP(13);
    float ga[8] = {0.f, 0.f, 0.f, 0.f, 0.f, 0.f, 0.f, 0.f};
#pragma unroll
    for (int i = 0; i < 9; ++i) {
      const float xf = __uint_as_float(xs[384 + (tid >> 3) + 64 * i]);
#pragma unroll
      for (int e = 0; e < 8; ++e) ga[e] = fmaf(xf, gf[i][e], ga[e]);
    }
    LAS float* gp = (LAS float*)(smem + P3_SOFF);
    {
      const float r0 = red32(ga[0], ga[4]), r1 = red32(ga[1], ga[5]), r2 = red32(ga[2], ga[6]), r3 = red32(ga[3], ga[7]);
      *(LAS u32x2*)(gp + (w * 2 + ((lane >> 3) & 1)) * 64 + h * 32 + (lane & 7) * 4 + ((lane >> 4) & 1) * 2) =
          u32x2{__float_as_uint(red16(r0, r2)), __float_as_uint(red16(r1, r3))};
    }
    LDS_BAR();
    STAMP(18);
    float av;
    {
      float s = 0.f;
#pragma unroll
      for (int i = 0; i < 16; ++i) s += gp[i * 64 + lane];
      s = rbf(s);
      auto sw = __builtin_amdgcn_permlane32_swap(__float_as_uint(s), __float_as_uint(s), false, false);
      const float gs = __uint_as_float(sw[0]), us = __uint_as_float(sw[1]);
      av = rbf(rbf(gs / (1.f + __expf(-gs))) * us);
    }
    float y[8] = {0.f, 0.f, 0.f, 0.f, 0.f, 0.f, 0.f, 0.f};
#pragma unroll
    for (int i = 0; i < 8; ++i) {
      const float a = __shfl(av, g * 8 + i);
#pragma unroll
      for (int e = 0; e < 8; ++e) y[e] = fmaf(a, df[i][e], y[e]);
    }
    const float r0 = red32(y[0], y[4]), r1 = red32(y[1], y[5]), r2 = red32(y[2], y[6]), r3 = red32(y[3], y[7]);
    const float y0 = red16(r0, r2), y1 = red16(r1, r3);
    STAMP(19);
    if (l16 < 9) gstore8s(ws, SY_PART + fs * 2304 + ((l16 < 8 ? w * 64 + l16 * 8 : 512 + w * 8) + g * 2) * 4, u32x2{nsent(__float_as_uint(y0)), nsent(__float_as_uint(y1))});
  }
  if (tok != sel && (w == 0 || isbest)) {
#pragma unroll
    for (int i = 0; i < 3; ++i) {
      const bool valid = (i < nnt) && ((nt0 + i) * 16 + 4 * g < 72);
      if (valid) *(u32x2*)(out + (size_t)tok * 1152 + (cg * 72 + (nt0 + i) * 16 + 4 * g) * 2) = ov[i];
    }
  }
  if (!ffn && b < 180 && w == 1) {
    const int c0 = (b - 144) * 16 + (lane & 3) * 4;
    float r = 0.f;
#pragma unroll
    for (int i = 0; i < 5; ++i) { const unsigned xv = xs[i * 64 + lane]; r = fmaf(bflo(xv), bflo(wrow[i]), fmaf(bfhi(xv), bfhi(wrow[i]), r)); }
    const unsigned n01 = xs[c0 >> 1], n23 = xs[(c0 >> 1) + 1];
    r = rbf(sum64(r));
    const float sig = rbf(1.f / (1.f + __expf(-r)));
    STAMP(13);
    u32x4 pv[9];
#pragma unroll
    for (int i = 0; i < 9; ++i) pv[i] = u32x4{SENT, SENT, SENT, SENT};
    u32x2 hp = u32x2{SENT, SENT};
    _Pragma("unroll 1") for (unsigned spin = 0; spin < SPIN_CAP; ++spin) {
      poll_part(pv, hp, ws, SY_PART + (lane >> 2) * 2304 + c0 * 4, SY_CH + wgrp * 1152 + c0 * 2);
      unsigned m = max(hp[0], hp[1]);
#pragma unroll
      for (int i = 0; i < 9; ++i) m = max(m, mx4(pv[i]));
      if (__all(m != SENT)) break;
    }
    float y[4];
#pragma unroll
    for (int e = 0; e < 4; ++e) {
      float t = 0.f;
#pragma unroll
      for (int i = 0; i < 9; ++i) t += __uint_as_float(pv[i][e]);
      t += dppf<0x124>(t); t += dppf<0x128>(t);
      y[e] = rbf(rbf(sum_x32(sum_x16(t))) * sig);
    }
    if (lane < 4) *(u32x2*)(out + (size_t)sel * 1152 + c0 * 2) =
        u32x2{cvtpk(bflo(hp[0]) + rbf(bflo(n01) + y[0]), bfhi(hp[0]) + rbf(bfhi(n01) + y[1])),
              cvtpk(bflo(hp[1]) + rbf(bflo(n23) + y[2]), bfhi(hp[1]) + rbf(bfhi(n23) + y[3]))};
    if (b == 144 && lane == 0) st_u64(ws + WS_EPOCH, tag);
  }
  STAMP_FLUSH(0);
}

extern "C" void kernel_launch(void* const* d_in, const int* in_sizes, int n_in, void* d_out, int out_size, void* d_ws,
                              size_t ws_size, hipStream_t stream) {
  (void)hipFuncSetAttribute((const void*)k_block, hipFuncAttributeMaxDynamicSharedMemorySize, LDS_BYTES);
  k_block<<<256, 512, LDS_BYTES, stream>>>((const char*)d_in[0], (const char*)d_in[1], (const char*)d_in[2], (const char*)d_in[4],
                                           (const char*)d_in[5], (const char*)d_in[6], (const char*)d_in[7], (char*)d_out, (char*)d_ws);
}
```

```cpp
#include <hip/hip_runtime.h>
#include <hip/hip_bf16.h>

typedef __attribute__((ext_vector_type(8))) short bf16x8;
typedef __attribute__((ext_vector_type(4))) short s16x4;
typedef __attribute__((ext_vector_type(16))) float f32x16;
typedef __attribute__((ext_vector_type(4))) float f32x4;
typedef __attribute__((ext_vector_type(4))) unsigned u32x4;
typedef __attribute__((ext_vector_type(2))) unsigned u32x2;
typedef __attribute__((ext_vector_type(2))) __bf16 b2_t;
typedef unsigned long long u64;
#define LAS __attribute__((address_space(3)))
#define GAS __attribute__((address_space(1)))
typedef LAS const char* lds_cptr;
typedef LAS char* lds_ptr;
typedef GAS u64 gu64;
typedef GAS unsigned gu32;

constexpr int S_ = 2048, D_ = 576, H_ = 32, F_ = 4608;
constexpr int CHUNK_B = 19456;
constexpr int HEAD_B = 8 * CHUNK_B;
constexpr int WS_EPOCH = 0;
constexpr int WS_FLAG1 = 4096;
constexpr int WS_KV = 8192;
constexpr int WS_Q1 = WS_KV + H_ * HEAD_B;
constexpr int WS_Q2 = WS_Q1 + H_ * 65536;
constexpr int WS_SENT = WS_Q2 + H_ * 8192;
constexpr int WS_O1 = WS_SENT;
constexpr int WS_O2 = WS_O1 + H_ * 65536;
constexpr int SY_STATS = WS_O2 + S_ * 128;
constexpr int SY_BEST = SY_STATS + 128 * 4096;
constexpr int SY_CHN = SY_BEST + 16 * 4096;
constexpr int SY_CH = SY_CHN + 128 * 1152;
constexpr int SY_PART = SY_CH + 128 * 1152;
constexpr int SY_END = SY_PART + 144 * 2304;
static_assert(SY_END - WS_SENT <= 256 * 16384, "sentinel area");
constexpr unsigned SENT = 0xFFFFFFFFu;

#define MFMA32(a, b, c) __builtin_amdgcn_mfma_f32_32x32x16_bf16(a, b, c, 0, 0, 0)
#define MFMA32K8(a, b, c) __builtin_amdgcn_mfma_f32_32x32x8bf16_1k(a, b, c, 0, 0, 0)
#define MFMA16(a, b, c) __builtin_amdgcn_mfma_f32_16x16x32_bf16(a, b, c, 0, 0, 0)
#define WAIT_VM(N) asm volatile("s_waitcnt vmcnt(" #N ")" ::: "memory")
#define WAIT_BAR(N) asm volatile("s_waitcnt vmcnt(" #N ") lgkmcnt(0)\n\ts_barrier" ::: "memory")
#define LDS_BAR() asm volatile("s_waitcnt lgkmcnt(0)\n\ts_barrier" ::: "memory")
#define PIN(x) asm volatile("" : "+v"(x))
#define SB() __builtin_amdgcn_sched_barrier(0)

__device__ __forceinline__ unsigned cvtpk(float lo, float hi) {
  typedef __attribute__((ext_vector_type(2))) float f2_t;
  return __builtin_bit_cast(unsigned, __builtin_convertvector((f2_t{lo, hi}), b2_t));
}
__device__ __forceinline__ float bflo(unsigned u) { return __uint_as_float(u << 16); }
__device__ __forceinline__ float bfhi(unsigned u) { return __uint_as_float(u & 0xffff0000u); }
__device__ __forceinline__ float rbf(float x) { return bflo(cvtpk(x, x)); }
__device__ __forceinline__ float dot2(unsigned a, unsigned b, float c) {
  return __builtin_amdgcn_fdot2_f32_bf16(__builtin_bit_cast(b2_t, a), __builtin_bit_cast(b2_t, b), c, false);
}
__device__ __forceinline__ void glds16(const void* sbase, unsigned voff, unsigned lds_dst) {
  unsigned sv;
  asm volatile("s_mov_b32 %0, m0\n\ts_mov_b32 m0, %3\n\ts_nop 0\n\tglobal_load_lds_dwordx4 %1, %2\n\ts_mov_b32 m0, %0"
               : "=&s"(sv) : "v"(voff), "s"(sbase), "s"(lds_dst) : "memory");
}
__device__ __forceinline__ u32x4 gload16(const void* sbase, unsigned voff) {
  u32x4 r; asm volatile("global_load_dwordx4 %0, %1, %2" : "=v"(r) : "v"(voff), "s"(sbase) : "memory"); return r;
}
__device__ __forceinline__ u32x2 gload8(const void* sbase, unsigned voff) {
  u32x2 r; asm volatile("global_load_dwordx2 %0, %1, %2" : "=v"(r) : "v"(voff), "s"(sbase) : "memory"); return r;
}
__device__ __forceinline__ u32x4 gload16s(const void* sbase, unsigned voff) {
  u32x4 r; asm volatile("global_load_dwordx4 %0, %1, %2 sc1" : "=v"(r) : "v"(voff), "s"(sbase) : "memory"); return r;
}
__device__ __forceinline__ u32x2 gload8s(const void* sbase, unsigned voff) {
  u32x2 r; asm volatile("global_load_dwordx2 %0, %1, %2 sc1" : "=v"(r) : "v"(voff), "s"(sbase) : "memory"); return r;
}
__device__ __forceinline__ unsigned gload4s(const void* sbase, unsigned voff) {
  unsigned r; asm volatile("global_load_dword %0, %1, %2 sc1" : "=v"(r) : "v"(voff), "s"(sbase) : "memory"); return r;
}
__device__ __forceinline__ void gstore16s(void* sbase, unsigned voff, u32x4 v) {
  asm volatile("global_store_dwordx4 %0, %1, %2 sc1\n\ts_nop 1" :: "v"(voff), "v"(v), "s"(sbase) : "memory");
}
__device__ __forceinline__ void gstore4s(void* sbase, unsigned voff, unsigned v) {
  asm volatile("global_store_dword %0, %1, %2 sc1" :: "v"(voff), "v"(v), "s"(sbase) : "memory");
}
__device__ __forceinline__ void gstore8s(void* sbase, unsigned voff, u32x2 v) {
  asm volatile("global_store_dwordx2 %0, %1, %2 sc1" :: "v"(voff), "v"(v), "s"(sbase) : "memory");
}
__device__ __forceinline__ unsigned gload4(const void* sbase, unsigned voff) {
  unsigned r; asm volatile("global_load_dword %0, %1, %2" : "=v"(r) : "v"(voff), "s"(sbase) : "memory"); return r;
}
__device__ __forceinline__ unsigned nsent(unsigned v) { return min(v, SENT - 1u); }
__device__ __forceinline__ unsigned mx4(u32x4 v) { return max(max(v[0], v[1]), max(v[2], v[3])); }
#define PL(W, R, M, A) "v_cmp_eq_u32 vcc, -1, %[" M "]\n\ts_and_saveexec_b64 %[sv], vcc\n\tglobal_load_" W " %[" R "], %[" A "], %[b] sc1\n\ts_mov_b64 exec, %[sv]\n\t"
#define PADD(N) "v_add_u32 %[t], " N ", %[t]\n\t"
#define PEND "s_waitcnt vmcnt(0)"
#define PL7 PL("dwordx4", "r0", "m0", "t") PADD("0x20000") PL("dwordx4", "r1", "m1", "t") PADD("0x20000") PL("dwordx4", "r2", "m2", "t") PADD("0x20000") \
            PL("dwordx4", "r3", "m3", "t") PADD("0x20000") PL("dwordx4", "r4", "m4", "t") PADD("0x20000") PL("dwordx4", "r5", "m5", "t") PADD("0x20000") \
            PL("dwordx4", "r6", "m6", "t")
#define PL7O [r0] "+v"(r[0]), [r1] "+v"(r[1]), [r2] "+v"(r[2]), [r3] "+v"(r[3]), [r4] "+v"(r[4]), [r5] "+v"(r[5]), [r6] "+v"(r[6]), [sv] "=&s"(sv), [t] "+v"(a0)
#define PL7I [m0] "v"(mx4(r[0])), [m1] "v"(mx4(r[1])), [m2] "v"(mx4(r[2])), [m3] "v"(mx4(r[3])), [m4] "v"(mx4(r[4])), [m5] "v"(mx4(r[5])), \
             [m6] "v"(mx4(r[6])), [b] "s"(sb)
#define PLD(R) PL("dword", R, R, "u")
__device__ __forceinline__ void poll_o8(u32x4 (&r)[9], unsigned (&d)[4], const void* sb, unsigned a0, unsigned a2) {
  u64 sv;
  asm volatile(PL7 PADD("0x20000") PL("dwordx4", "r7", "m7", "t") PLD("d0") "v_add_u32 %[u], 0x2000, %[u]\n\t" PLD("d1") "v_add_u32 %[u], 0x2000, %[u]\n\t"
               PLD("d2") "v_add_u32 %[u], 0x2000, %[u]\n\t" PLD("d3") PEND
               : PL7O, [r7] "+v"(r[7]), [u] "+v"(a2), [d0] "+v"(d[0]), [d1] "+v"(d[1]), [d2] "+v"(d[2]), [d3] "+v"(d[3])
               : PL7I, [m7] "v"(mx4(r[7])) : "vcc", "memory");
}
__device__ __forceinline__ void poll_4x16(u32x4 (&r)[4], const void* sb, unsigned a0) {
  u64 sv;
  asm volatile(PL("dwordx4", "r0", "m0", "t") PADD("0x400") PL("dwordx4", "r1", "m1", "t") PADD("0x400") PL("dwordx4", "r2", "m2", "t") PADD("0x400")
               PL("dwordx4", "r3", "m3", "t") PEND
               : [r0] "+v"(r[0]), [r1] "+v"(r[1]), [r2] "+v"(r[2]), [r3] "+v"(r[3]), [sv] "=&s"(sv), [t] "+v"(a0)
               : [m0] "v"(mx4(r[0])), [m1] "v"(mx4(r[1])), [m2] "v"(mx4(r[2])), [m3] "v"(mx4(r[3])), [b] "s"(sb) : "vcc", "memory");
}
__device__ __forceinline__ void poll_8(u32x2& r, const void* sb, unsigned a0) {
  u64 sv;
  asm volatile(PL("dwordx2", "r0", "m0", "a0") PEND
               : [r0] "+v"(r), [sv] "=&s"(sv) : [m0] "v"(max(r[0], r[1])), [a0] "v"(a0), [b] "s"(sb) : "vcc", "memory");
}
__device__ __forceinline__ void poll_5x4(unsigned (&r)[5], const void* sb, unsigned a0, unsigned a4) {
  u64 sv;
  asm volatile(PL("dword", "r0", "r0", "t") PADD("0x100") PL("dword", "r1", "r1", "t") PADD("0x100") PL("dword", "r2", "r2", "t") PADD("0x100")
               PL("dword", "r3", "r3", "t") PL("dword", "r4", "r4", "a4") PEND
               : [r0] "+v"(r[0]), [r1] "+v"(r[1]), [r2] "+v"(r[2]), [r3] "+v"(r[3]), [r4] "+v"(r[4]), [sv] "=&s"(sv), [t] "+v"(a0)
               : [a4] "v"(a4), [b] "s"(sb) : "vcc", "memory");
}
#define PL9(STEP) PL("dwordx4", "r0", "m0", "t") PADD(STEP) PL("dwordx4", "r1", "m1", "t") PADD(STEP) PL("dwordx4", "r2", "m2", "t") PADD(STEP) \
            PL("dwordx4", "r3", "m3", "t") PADD(STEP) PL("dwordx4", "r4", "m4", "t") PADD(STEP) PL("dwordx4", "r5", "m5", "t") PADD(STEP) \
            PL("dwordx4", "r6", "m6", "t") PADD(STEP) PL("dwordx4", "r7", "m7", "t") PADD(STEP) PL("dwordx4", "r8", "m8", "t")
__device__ __forceinline__ void poll_part(u32x4 (&r)[9], u32x2& hp, const void* sb, unsigned a0, unsigned a1) {
  u64 sv;
  asm volatile(PL("dwordx2", "hp", "mh", "a1") PL9("0x9000") PEND
               : PL7O, [r7] "+v"(r[7]), [r8] "+v"(r[8]), [hp] "+v"(hp)
               : PL7I, [m7] "v"(mx4(r[7])), [m8] "v"(mx4(r[8])), [mh] "v"(max(hp[0], hp[1])), [a1] "v"(a1) : "vcc", "memory");
}
__device__ __forceinline__ u32x4 gload16s_w(const void* sbase, unsigned voff) {
  u32x4 r; asm volatile("global_load_dwordx4 %0, %1, %2 sc1\n\ts_waitcnt vmcnt(0)" : "=&v"(r) : "v"(voff), "s"(sbase) : "memory"); return r;
}
__device__ __forceinline__ bf16x8 lds128(lds_cptr p) { return *(const LAS bf16x8*)p; }
__device__ __forceinline__ s16x4 ldstr(lds_cptr p) { return __builtin_amdgcn_ds_read_tr16_b64_v4i16((LAS s16x4*)p); }
__device__ __forceinline__ bf16x8 cat8(s16x4 a, s16x4 b) { return __builtin_shufflevector(a, b, 0, 1, 2, 3, 4, 5, 6, 7); }
__device__ __forceinline__ void st_u64(char* p, u64 v) { __hip_atomic_store((gu64*)p, v, __ATOMIC_RELAXED, __HIP_MEMORY_SCOPE_AGENT); }
__device__ __forceinline__ u64 ld_u64(const char* p) { return __hip_atomic_load((gu64*)p, __ATOMIC_RELAXED, __HIP_MEMORY_SCOPE_AGENT); }
constexpr unsigned SPIN_CAP = 1u << 18;
template <int CTRL> __device__ __forceinline__ unsigned dppu(unsigned x) { return (unsigned)__builtin_amdgcn_mov_dpp((int)x, CTRL, 0xf, 0xf, true); }
template <int CTRL> __device__ __forceinline__ float dppf(float x) { return __uint_as_float(dppu<CTRL>(__float_as_uint(x))); }
__device__ __forceinline__ float sum_x16(float x) { auto s = __builtin_amdgcn_permlane16_swap(__float_as_uint(x), __float_as_uint(x), false, false); return __uint_as_float(s[0]) + __uint_as_float(s[1]); }
__device__ __forceinline__ float sum_x32(float x) { auto s = __builtin_amdgcn_permlane32_swap(__float_as_uint(x), __float_as_uint(x), false, false); return __uint_as_float(s[0]) + __uint_as_float(s[1]); }
__device__ __forceinline__ float red32(float a, float b) { auto s = __builtin_amdgcn_permlane32_swap(__float_as_uint(a), __float_as_uint(b), false, false); return __uint_as_float(s[0]) + __uint_as_float(s[1]); }
__device__ __forceinline__ float red16(float a, float b) { auto s = __builtin_amdgcn_permlane16_swap(__float_as_uint(a), __float_as_uint(b), false, false); return __uint_as_float(s[0]) + __uint_as_float(s[1]); }
__device__ __forceinline__ float sum8(float x) { x += dppf<0xB1>(x); x += dppf<0x4E>(x); x += dppf<0x141>(x); return x; }
__device__ __forceinline__ float sum64(float x) { x = sum8(x); x += dppf<0x140>(x); return sum_x32(sum_x16(x)); }
__device__ __forceinline__ unsigned umax64(unsigned x) {
  x = max(x, dppu<0xB1>(x)); x = max(x, dppu<0x4E>(x)); x = max(x, dppu<0x141>(x)); x = max(x, dppu<0x140>(x));
  auto s = __builtin_amdgcn_permlane16_swap(x, x, false, false); x = max(s[0], s[1]);
  auto t = __builtin_amdgcn_permlane32_swap(x, x, false, false); return max(t[0], t[1]);
}
#define STAMP_DECL
#define STAMP(i)
#define CSTAMP(i)
#define STAMP_FLUSH(kid)

constexpr int P1_APITCH = 1168;
constexpr int P1_WBUF = 14336;
constexpr int P1_WOFF = 64 * P1_APITCH;
constexpr int LDS_BYTES = 163840;
static_assert(P1_WOFF + 6 * P1_WBUF <= LDS_BYTES, "P1 LDS");
__device__ __forceinline__ unsigned w_voff(int i, int j) {
  const int R = i / 27, cc = i - R * 27;
  return ((R & ~15) + 4 * (R & 3) + ((R >> 2) & 3)) * 3456 + j * 432 + cc * 16;
}
template <int N> __device__ __forceinline__ void w_dma(const char* wq, int c, int rot, const unsigned (&voff)[2], unsigned dst) {
  int kc = c + rot; kc = kc >= 18 ? kc - 18 : kc;
#pragma unroll
  for (int p = 0; p < N; ++p) glds16(wq + (size_t)kc * (32 * 3456), voff[p], dst + (c % 6) * P1_WBUF + p * 1024);
}
template <int V> __device__ __forceinline__ void wait_vm() { asm volatile("s_waitcnt vmcnt(%0)" :: "n"(V) : "memory"); }
template <int N, bool MM>
__device__ __forceinline__ void p1_main(f32x16 (&acc)[2], lds_cptr ap, lds_cptr bp, const char* wq, int rot, const unsigned (&voff)[2],
                                        unsigned dst, char* ws, unsigned soff) {
  bf16x8 wf[2], xa[2], xb[2];
  auto frags = [&](int c) {
    int kc = c + rot; kc = kc >= 18 ? kc - 18 : kc;
#pragma unroll
    for (int ks = 0; ks < 2; ++ks) {
      wf[ks] = cat8(ldstr(bp + (c % 6) * P1_WBUF + ks * 6912), ldstr(bp + (c % 6) * P1_WBUF + ks * 6912 + 432));
      xa[ks] = lds128(ap + kc * 64 + ks * 32); xb[ks] = lds128(ap + 32 * P1_APITCH + kc * 64 + ks * 32);
    }
  };
  wait_vm<5 * N>();
  LDS_BAR();
  if (MM) frags(0);
#pragma unroll
  for (int c = 0; c < 18; ++c) {
    if (N) { if (c == 0) wait_vm<4 * N>(); else if (c <= 13) wait_vm<3 * N>(); else if (c == 14) wait_vm<2 * N>(); else if (c == 15) wait_vm<N>(); else if (c == 16) wait_vm<0>(); }
    if (c < 17) asm volatile("s_barrier" ::: "memory");
    const bf16x8 w0 = wf[0], w1 = wf[1], a0 = xa[0], a1 = xa[1], b0 = xb[0], b1 = xb[1];
    int kn = c + 1 + rot; kn = kn >= 18 ? kn - 18 : kn;
    const lds_cptr bn = bp + ((c + 1) % 6) * P1_WBUF, an = ap + kn * 64;
    const bool rd = MM && c < 17;
    SB();
    if (MM) acc[0] = MFMA32(w0, a0, acc[0]);
    SB();
    if (rd) { wf[0] = cat8(ldstr(bn), ldstr(bn + 432)); xa[0] = lds128(an); xb[0] = lds128(an + 32 * P1_APITCH); }
    SB();
    if (MM) acc[1] = MFMA32(w0, b0, acc[1]);
    SB();
    if (rd) { wf[1] = cat8(ldstr(bn + 6912), ldstr(bn + 6912 + 432)); xa[1] = lds128(an + 32); xb[1] = lds128(an + 32 * P1_APITCH + 32); }
    SB();
    if (MM) acc[0] = MFMA32(w1, a1, acc[0]);
    SB();
    if (c >= 1 && c <= 12) w_dma<N>(wq, c + 5, rot, voff, dst);
    if (N == 0 && c < 8) { gstore16s(ws + c * 2048, soff, u32x4{SENT, SENT, SENT, SENT}); gstore16s(ws + c * 2048 + 1024, soff, u32x4{SENT, SENT, SENT, SENT}); }
    SB();
    if (MM) acc[1] = MFMA32(w1, b1, acc[1]);
    SB();
  }
}

struct AttnState { f32x16 sA, oA, oB; u32x4 pend; bf16x8 v1p, k1, k1n, v0, v1; s16x4 k2, k2n; };
struct QFrag { bf16x8 q1; s16x4 q2[4]; };
#define EXP2(x) __builtin_amdgcn_exp2f(x)
#define G4(P, i, S, r) do { P[i] = cvtpk(EXP2(S[r]), EXP2(S[(r) + 1])); P[(i) + 1] = cvtpk(EXP2(S[(r) + 2]), EXP2(S[(r) + 3])); } while (0)
__device__ __forceinline__ s16x4 ldk2(lds_cptr p) { return __builtin_bit_cast(s16x4, u32x2{*(const LAS unsigned*)p, *(const LAS unsigned*)(p + 128)}); }
__device__ __forceinline__ bf16x8 ldv(lds_cptr p) { return cat8(ldstr(p), ldstr(p + 320)); }
__device__ __forceinline__ void attn_prime(AttnState& st, lds_cptr kp, lds_cptr k2p, lds_cptr vp, const QFrag& qa) {
  st.k1 = lds128(kp); st.k2 = ldk2(k2p); st.k1n = lds128(kp + 1024); st.v0 = ldv(vp); st.v1 = ldv(vp + 640);
  st.sA = MFMA32K8(st.k2, qa.q2[0], MFMA32(st.k1, qa.q1, f32x16{}));
}
template <int PR, class HOOK>
__device__ __forceinline__ void attn8(AttnState& st, lds_cptr kp, lds_cptr k2p, lds_cptr vp, int nx, const QFrag& qa, const QFrag& qb, HOOK hook) {
#pragma unroll
  for (int t = 0; t < 8; ++t) {
    u32x4 p0, p1; f32x16 sB;
    const int pr = PR == 0 ? ((t & 1) ? -1 : 3 - (t >> 1)) : (t & 3) ? -1 : (PR == 1 ? 3 : 1) - (t >> 2);
    if (pr == 0) __builtin_amdgcn_s_setprio(0);
    if (pr == 1) __builtin_amdgcn_s_setprio(1);
    if (pr == 2) __builtin_amdgcn_s_setprio(2);
    if (pr == 3) __builtin_amdgcn_s_setprio(3);
    __builtin_amdgcn_s_waitcnt(0xC07F);
    const bf16x8 v0n = ldv(t < 7 ? vp + (t + 1) * 1280 : vp + nx), v1n = ldv(t < 7 ? vp + (t + 1) * 1280 + 640 : vp + nx + 640);
    const bf16x8 k1nn = lds128(t < 6 ? kp + (t + 2) * 1024 : kp + nx + (t - 6) * 1024);
    if (t == 2) st.k2n = ldk2(k2p + 512);
    if (t == 6) st.k2n = ldk2(k2p + nx);
    hook(t);
    SB();
    sB = MFMA32(st.k1, qb.q1, f32x16{}); SB();
    G4(p0, 0, st.sA, 0); SB();
    st.oB = MFMA32(st.v1p, __builtin_bit_cast(bf16x8, st.pend), st.oB); SB();
    G4(p0, 2, st.sA, 4); SB();
    sB = MFMA32K8(st.k2, qb.q2[t & 3], sB); SB();
    G4(p1, 0, st.sA, 8); SB();
    st.oA = MFMA32(st.v0, __builtin_bit_cast(bf16x8, p0), st.oA); SB();
    G4(p1, 2, st.sA, 12); SB();
    st.sA = MFMA32(st.k1n, qa.q1, f32x16{}); SB();
    G4(p0, 0, sB, 0); SB();
    st.oA = MFMA32(st.v1, __builtin_bit_cast(bf16x8, p1), st.oA); SB();
    G4(p0, 2, sB, 4); SB();
    if ((t & 3) == 3) st.k2 = st.k2n;
    st.sA = MFMA32K8(st.k2, qa.q2[(t + 1) & 3], st.sA); SB();
    G4(p1, 0, sB, 8); SB();
    st.oB = MFMA32(st.v0, __builtin_bit_cast(bf16x8, p0), st.oB); SB();
    G4(p1, 2, sB, 12); SB();
    st.pend = p1; st.v1p = st.v1; st.k1 = st.k1n; st.k1n = k1nn; st.v0 = v0n; st.v1 = v1n;
  }
  PIN(st.oA); PIN(st.oB); PIN(st.pend); PIN(st.sA);
}
__device__ __forceinline__ int kv_goff(int u) {
  const int slot = u / 19, pc = u - slot * 19;
  return ((slot & 1) * 4 + (slot >> 1)) * CHUNK_B + pc * 1024;
}
constexpr int P2_XOFF = 82944;
constexpr int P3_XOFF = 116736;
constexpr int FB_OFF = 137216;
constexpr int P3_SOFF = 147456;

__global__ void __launch_bounds__(512, 2) k_block(const char* __restrict__ x, const char* __restrict__ wqkv, const char* __restrict__ wo,
                                                  const char* __restrict__ wr, const char* __restrict__ wg, const char* __restrict__ wu,
                                                  const char* __restrict__ wd, char* __restrict__ out, char* __restrict__ ws) {
  extern __shared__ __attribute__((aligned(16))) char smem[];
  const int tid = threadIdx.x, lane = tid & 63, w = __builtin_amdgcn_readfirstlane(tid >> 6);
  const int b = blockIdx.x, xc = b & 7, sl = b >> 3;
  const unsigned lds0 = (unsigned)(uintptr_t)smem;
  const int h = lane >> 5, r32 = lane & 31;
  STAMP_DECL STAMP(0);
  const char* pc0_ = (const char*)__builtin_amdgcn_s_getpc();
  u64 tag;
  {
    const int j = 4 * (xc >> 2) + (sl >> 3), rb = 8 * (xc & 3) + (sl & 7);
    const int row = tid >> 3, j8 = tid & 7;
    u32x4 xr[9];
    {
      const char* xb = x + (size_t)rb * 64 * 1152;
      const unsigned vo = row * 1152 + j8 * 16;
#pragma unroll
      for (int i = 0; i < 9; ++i) xr[i] = gload16(xb + i * 128, vo);
    }
    u32x2 ep = gload8s(ws, WS_EPOCH);
    u32x4 cpf = u32x4{0, 0, 0, 0};
    if (w == 0) cpf = gload16(pc0_, (sl + 32 * (lane >> 5)) * 768 + r32 * 24);
    const int rot = ((sl & 7) * 9) >> 2;
    const int pc0 = 2 * max(w - 1, 0);
    unsigned voff[2];
#pragma unroll
    for (int p = 0; p < 2; ++p) voff[p] = w_voff(min((pc0 + p) * 64 + lane, 863), j);
    const unsigned wdst = lds0 + P1_WOFF + pc0 * 1024;
    asm volatile("s_barrier" ::: "memory");
    if (w) {
#pragma unroll
      for (int c = 0; c < 3; ++c) w_dma<2>(wqkv, c, rot, voff, wdst);
      WAIT_VM(6);
    } else {
      WAIT_VM(0);
    }
#pragma unroll
    for (int i = 0; i < 9; ++i) PIN(xr[i]);
    PIN(ep); PIN(cpf);
    tag = (((u64)ep[1] << 32) | ep[0]) * 6364136223846793005ull + 1442695040888963407ull;
    STAMP(1);
    {
      float s = 0.f, q = 0.f;
#pragma unroll
      for (int i = 0; i < 9; ++i)
#pragma unroll
        for (int e = 0; e < 4; ++e) { s = dot2(xr[i][e], 0x3F803F80u, s); q = dot2(xr[i][e], xr[i][e], q); }
      if (w) {
#pragma unroll
        for (int c = 3; c < 6; ++c) w_dma<2>(wqkv, c, rot, voff, wdst);
      }
      s = sum8(s); q = sum8(q);
      const float m = s * (1.f / 576.f);
      const float var = fmaxf(fmaf(q, 1.f / 576.f, -m * m), 0.f);
      const float rinv = 1.f / rbf(sqrtf(rbf(rbf(var) + 1e-5f)));
      const float nb = -rbf(m) * rinv;
      const lds_ptr ap = (lds_ptr)smem + row * P1_APITCH + j8 * 16;
#pragma unroll
      for (int i = 0; i < 9; ++i) {
        u32x4 o;
#pragma unroll
        for (int e = 0; e < 4; ++e) o[e] = cvtpk(fmaf(bflo(xr[i][e]), rinv, nb), fmaf(bfhi(xr[i][e]), rinv, nb));
        *(LAS u32x4*)(ap + i * 128) = o;
      }
    }
    STAMP(2); CSTAMP(2);
    f32x16 acc[2]; acc[0] = f32x16{}; acc[1] = f32x16{};
    {
      const int sg = 4 * ((lane >> 4) & 1) + (lane & 3);
      const int col = w < 6 ? 36 * w + 4 * ((0x86753120u >> (4 * sg)) & 15) : 36 * min(sg, 5) + 16;
      const lds_cptr ap = (lds_cptr)smem + r32 * P1_APITCH + h * 16;
      const lds_cptr bp = (lds_cptr)smem + P1_WOFF + (4 * ((lane & 15) >> 2) + 2 * h) * 432 + col * 2;
      const unsigned soff = WS_SENT + b * 16384 + lane * 16;
      if (w == 7) p1_main<2, false>(acc, ap, bp, wqkv, rot, voff, wdst, ws, soff);
      else if (w == 0) p1_main<0, true>(acc, ap, bp, wqkv, rot, voff, wdst, ws, soff);
      else p1_main<2, true>(acc, ap, bp, wqkv, rot, voff, wdst, ws, soff);
    }
    STAMP(3);
    constexpr float QS = 0.23570226039551584f * 1.4426950408889634f;
    const int t0 = 2 * rb;
    const int kvt = WS_KV + (t0 >> 3) * CHUNK_B;
    const lds_ptr vst = (lds_ptr)smem + P1_WOFF + (r32 >> 2) * 160;
    if (w < 6) {
      const int P = 6 * j + w, ty = P >> 4, hd = 2 * (P & 15);
      const float sc = ty ? 1.f : QS;
      const int base = (ty ? kvt + hd * HEAD_B + (t0 & 7) * 1024 : WS_Q1 + hd * 65536 + t0 * 1024) + lane * 16;
      const int hs = ty ? HEAD_B : 65536;
#pragma unroll
      for (int mi = 0; mi < 2; ++mi)
#pragma unroll
        for (int ab = 0; ab < 2; ++ab) {
          u32x4 o;
#pragma unroll
          for (int e = 0; e < 4; ++e) o[e] = cvtpk(acc[mi][8 * ab + 2 * e] * sc, acc[mi][8 * ab + 2 * e + 1] * sc);
          if (ty == 2) *(LAS u32x4*)(vst + (2 * w + ab) * 2560 + mi * 1280 + (r32 & 3) * 32 + h * 16) = o;
          else gstore16s(ws, base + ab * hs + mi * 1024, o);
        }
    } else if (w == 6) {
#pragma unroll
      for (int rq = 0; rq < 3; ++rq) {
        const int P = 6 * j + 2 * rq, ty = P >> 4, hd = 2 * ((P + h) & 15);
        const float sc = ty ? 1.f : QS;
#pragma unroll
        for (int mi = 0; mi < 2; ++mi) {
          const unsigned oa = cvtpk(acc[mi][4 * rq] * sc, acc[mi][4 * rq + 1] * sc), ob = cvtpk(acc[mi][4 * rq + 2] * sc, acc[mi][4 * rq + 3] * sc);
          if (ty == 2) {
            const lds_ptr d = vst + (4 * rq + 2 * h) * 2560 + mi * 1280 + 128 + (r32 & 3) * 8;
            *(LAS u32x2*)d = u32x2{oa, 0x00003F80u}; *(LAS u32x2*)(d + 2560) = u32x2{ob, 0x00003F80u};
          } else {
            const int base = (ty ? kvt + hd * HEAD_B + 8192 + (t0 & 7) * 128 : WS_Q2 + hd * 8192 + t0 * 128) + mi * 128 + r32 * 4;
            gstore4s(ws, base, oa); gstore4s(ws, base + (ty ? HEAD_B : 8192), ob);
          }
        }
      }
    }
    if (j >= 5) {
      LDS_BAR();
#pragma unroll
      for (int i = 0; i < 4; ++i) {
        const int pi = i * 512 + tid + (j == 5 ? 640 : 0), hh = pi / 160;
        if (pi < 1920) gstore16s(ws, kvt + (12 * j + hh - 64) * HEAD_B + 9216 + (t0 & 7) * 1280 + (pi - hh * 160) * 16,
                                 *(const LAS u32x4*)(smem + P1_WOFF + pi * 16));
      }
    }
    WAIT_BAR(0);
    if (tid == 0) st_u64(ws + WS_FLAG1 + (j * 32 + rb) * 8, tag);
    STAMP(4); CSTAMP(3);
  }
  const int panel = 4 * xc + (sl & 3), cg = sl >> 2;
  const int mt = w & 3, kq = w >> 2, g = lane >> 4, l16 = lane & 15;
  const int nnt = kq ? 2 : 3, nt0 = kq ? 3 : 0;
  const int tok = panel * 64 + mt * 16 + l16;
  const bool ffn = b < 144;
  const int fs = ((b & 7) + 8 * (b >> 4)) * 2 + ((b >> 3) & 1);
  u32x2 xres[3], wrr[3], gw[9], uw[9];
  unsigned wrow[5];
  u32x4 dw[8];
  {
    const int head = 4 * xc + (sl & 3), qp = sl >> 2;
    const int qpair = w & 3, kh = w >> 2;
    if (w == 0) {
      const char* f1 = ws + WS_FLAG1 + ((h ? (64 + head) / 12 : (32 + head) / 12) * 32 + r32) * 8;
      const char* f2 = ws + WS_FLAG1 + ((head / 12) * 32 + 4 * qp + (lane & 3)) * 8;
      _Pragma("unroll 1") for (unsigned spin = 0; spin < SPIN_CAP; ++spin) {
        const u64 a = ld_u64(f1), c = ld_u64(f2);
        if (__all(a == tag && c == tag)) break;
        __builtin_amdgcn_s_sleep(1);
      }
    }
    LDS_BAR();
    STAMP(5);
    const char* img = ws + WS_KV + (size_t)head * HEAD_B;
    const int qt = qp * 8 + 2 * qpair;
    u32x4 kv[19];
#pragma unroll
    for (int i = 0; i < 5; ++i) kv[i] = gload16s(img + kv_goff(8 * i + w), lane * 16);
    u32x4 q1a = gload16s(ws, WS_Q1 + head * 65536 + qt * 1024 + lane * 16);
    u32x4 q1b = gload16s(ws, WS_Q1 + head * 65536 + qt * 1024 + 1024 + lane * 16);
    unsigned q2a = gload4s(ws, WS_Q2 + head * 8192 + qt * 128 + r32 * 4);
    unsigned q2b = gload4s(ws, WS_Q2 + head * 8192 + qt * 128 + 128 + r32 * 4);
#pragma unroll
    for (int i = 5; i < 8; ++i) kv[i] = gload16s(img + kv_goff(8 * i + w), lane * 16);
    const lds_ptr kdst = (lds_ptr)smem + w * 1024 + lane * 16;
    WAIT_VM(3);
#pragma unroll
    for (int i = 0; i < 5; ++i) { PIN(kv[i]); *(LAS u32x4*)(kdst + i * 8192) = kv[i]; }
    PIN(q1a); PIN(q1b); PIN(q2a); PIN(q2b);
    QFrag qa, qb;
    qa.q1 = __builtin_bit_cast(bf16x8, q1a); qb.q1 = __builtin_bit_cast(bf16x8, q1b);
#pragma unroll
    for (int i = 0; i < 4; ++i) {
      const unsigned ua = h == (i >> 1) ? q2a : 0u, ub = h == (i >> 1) ? q2b : 0u;
      qa.q2[i] = __builtin_bit_cast(s16x4, (i & 1) ? u32x2{0u, ua} : u32x2{ua, 0u});
      qb.q2[i] = __builtin_bit_cast(s16x4, (i & 1) ? u32x2{0u, ub} : u32x2{ub, 0u});
    }
    const int vlo = h * 160 + ((lane & 16) ? 128 + (l16 >> 2) * 8 : l16 * 8);
    const lds_cptr kp = (lds_cptr)smem + kh * CHUNK_B + lane * 16;
    const lds_cptr k2p = (lds_cptr)smem + kh * CHUNK_B + 8192 + h * 256 + r32 * 4;
    const lds_cptr vp = (lds_cptr)smem + kh * CHUNK_B + 9216 + vlo;
    LDS_BAR();
    STAMP(6); CSTAMP(0);
    AttnState st;
    st.oA = f32x16{}; st.oB = f32x16{}; st.pend = u32x4{0, 0, 0, 0}; st.v1p = bf16x8{};
    attn_prime(st, kp, k2p, vp, qa);
    attn8<0>(st, kp, k2p, vp, 0, qa, qb, [&](int t) {
#pragma unroll
      for (int i = 8 + 3 * t; i < 11 + 3 * t; ++i) if (i < 19) kv[i] = gload16s(img + kv_goff(8 * i + w), lane * 16);
    });
    STAMP(14);
    WAIT_VM(0);
#pragma unroll
    for (int i = 5; i < 19; ++i) { PIN(kv[i]); *(LAS u32x4*)(kdst + i * 8192) = kv[i]; }
    LDS_BAR();
    STAMP(15);
    attn_prime(st, kp + 2 * CHUNK_B, k2p + 2 * CHUNK_B, vp + 2 * CHUNK_B, qa);
    const int ycol = l16 < 8 ? w * 64 + l16 * 8 : 512 + w * 8;
    const unsigned dn = ffn ? (fs * 32 + g * 8) * 1152 + ycol * 2 : 0u;
    const unsigned go = ffn ? (tid >> 3) * (F_ * 2) + fs * 64 + (tid & 7) * 8 : 0u;
    attn8<1>(st, kp + 2 * CHUNK_B, k2p + 2 * CHUNK_B, vp + 2 * CHUNK_B, 2 * CHUNK_B, qa, qb, [&](int t) {
      gw[t] = gload8(wg + t * 64 * (F_ * 2), go);
      if (t == 7) gw[8] = gload8(wg + 8 * 64 * (F_ * 2), go);
      if (t == 1 || t == 3 || t == 5) dw[t >> 1] = gload16(wd + (t >> 1) * 1152, dn);
    });
    STAMP(16);
    attn8<2>(st, kp + 4 * CHUNK_B, k2p + 4 * CHUNK_B, vp + 4 * CHUNK_B, 2 * CHUNK_B, qa, qb, [&](int t) {
      uw[t] = gload8(wu + t * 64 * (F_ * 2), go);
      if (t == 7) uw[8] = gload8(wu + 8 * 64 * (F_ * 2), go);
      if (t == 1 || t == 3 || t == 5) dw[3 + (t >> 1)] = gload16(wd + (3 + (t >> 1)) * 1152, dn);
      if (t < 3) {
        const int n = min((nt0 + t) * 16 + 4 * g, 68);
        xres[t] = gload8(x, tok * 1152 + (cg * 72 + n) * 2);
        wrr[t] = gload8(wr, (cg * 72 + n) * 2);
      } else {
        wrow[t - 3] = gload4(wr, min((t - 3) * 64 + lane, 287) * 4);
      }
    });
    STAMP(17);
    u32x4 fa, fb;
    LDS_BAR();
    attn8<0>(st, kp + 6 * CHUNK_B, k2p + 6 * CHUNK_B, vp + 6 * CHUNK_B, 0, qa, qb, [&](int t) {
#pragma unroll
      for (int i = 3 * t; i < 3 * t + 3; ++i) {
        if (i < 11) {
          const int pc = i < 10 ? 8 * i + w : 80;
          const int ci = pc * 64 + lane;
          const int R = ci / 9, cc = ci - R * 9;
          glds16(wo, R * 1152 + cg * 144 + cc * 16, lds0 + pc * 1024);
        }
      }
      if (t < 2) dw[6 + t] = gload16(wd + (6 + t) * 1152, dn);
      if (t == 4) { const int fo = w ? 0 : lane * 32; fa = gload16s(ws, WS_FLAG1 + fo); fb = gload16s(ws, WS_FLAG1 + fo + 16); }
    });
    st.oB = MFMA32(st.v1p, __builtin_bit_cast(bf16x8, st.pend), st.oB);
    STAMP(7); CSTAMP(1);
    LAS f32x4* xw = (LAS f32x4*)(smem + P2_XOFF) + (qpair * 2 + kh) * 192 + lane;
    LAS f32x4* xr_ = (LAS f32x4*)(smem + P2_XOFF) + (qpair * 2 + (kh ^ 1)) * 192 + lane;
    f32x16 o = kh ? st.oB : st.oA;
    {
      const f32x16 give = kh ? st.oA : st.oB;
#pragma unroll
      for (int r = 0; r < 3; ++r) xw[r * 64] = f32x4{give[4 * r], give[4 * r + 1], give[4 * r + 2], give[4 * r + 3]};
    }
    WAIT_VM(0);
    PIN(fa); PIN(fb);
    if (w == 0) _Pragma("unroll 1") for (unsigned spin = 0; spin < SPIN_CAP; ++spin) {
      const unsigned t0 = (unsigned)tag, t1 = (unsigned)(tag >> 32);
      if (__all(fa[0] == t0 && fa[1] == t1 && fa[2] == t0 && fa[3] == t1 && fb[0] == t0 && fb[1] == t1 && fb[2] == t0 && fb[3] == t1)) break;
      fa = gload16s_w(ws, WS_FLAG1 + lane * 32); fb = gload16s_w(ws, WS_FLAG1 + lane * 32 + 16);
    }
    WAIT_BAR(0);
#pragma unroll
    for (int r = 0; r < 3; ++r) {
      const f32x4 t = xr_[r * 64];
#pragma unroll
      for (int e = 0; e < 4; ++e) o[4 * r + e] += t[e];
    }
    float l;
    {
      auto sw = __builtin_amdgcn_permlane32_swap(__float_as_uint(o[10]), __float_as_uint(o[10]), false, false);
      l = __uint_as_float(sw[0]);
    }
    bool ok = (l > 8.6736174e-19f) && (l < 1.1529215e18f);
#pragma unroll
    for (int r = 0; r < 10; ++r) ok = ok && ((h && r >= 8) || fabsf(o[r]) < 3.0e38f);
    if (!__all(ok)) {
      const bf16x8 qm1 = kh ? qb.q1 : qa.q1; const s16x4 qm2 = kh ? qb.q2[0] : qa.q2[0];
      const lds_ptr fb = (lds_ptr)smem + FB_OFF + w * 1280;
      float mx = 0.f;
#pragma unroll 1
      for (int pass = 0; pass < 2; ++pass) {
        f32x16 cin;
#pragma unroll
        for (int r = 0; r < 16; ++r) cin[r] = -mx;
        float m2 = -INFINITY;
        o = f32x16{};
#pragma unroll 1
        for (int kt = 0; kt < 64; ++kt) {
          const char* cb = img + (kt >> 3) * CHUNK_B;
          u32x4 k1 = gload16s(cb + (kt & 7) * 1024, lane * 16);
          unsigned k2 = gload4s(cb + 8192 + (kt & 7) * 128, r32 * 4);
          u32x4 va = gload16s(cb + 9216 + (kt & 7) * 1280, lane * 16), vb = gload16s(cb + 9216 + (kt & 7) * 1280 + 1024, l16 * 16);
          WAIT_VM(0); PIN(k1); PIN(k2); PIN(va); PIN(vb);
          *(LAS u32x4*)(fb + lane * 16) = va; *(LAS u32x4*)(fb + 1024 + l16 * 16) = vb;
          f32x16 s = MFMA32(__builtin_bit_cast(bf16x8, k1), qm1, cin);
          s = MFMA32K8(__builtin_bit_cast(s16x4, (u32x2{k2, 0u})), qm2, s);
#pragma unroll
          for (int r = 0; r < 16; ++r) m2 = fmaxf(m2, s[r]);
          u32x4 p0, p1;
          G4(p0, 0, s, 0); G4(p0, 2, s, 4); G4(p1, 0, s, 8); G4(p1, 2, s, 12);
          asm volatile("s_waitcnt lgkmcnt(0)" ::: "memory");
          o = MFMA32(ldv((lds_cptr)fb + vlo), __builtin_bit_cast(bf16x8, p0), o);
          o = MFMA32(ldv((lds_cptr)fb + vlo + 640), __builtin_bit_cast(bf16x8, p1), o);
          asm volatile("s_waitcnt lgkmcnt(0)" ::: "memory");
        }
        auto sw = __builtin_amdgcn_permlane32_swap(__float_as_uint(m2), __float_as_uint(m2), false, false);
        mx = fmaxf(__uint_as_float(sw[0]), __uint_as_float(sw[1]));
      }
      auto sw = __builtin_amdgcn_permlane32_swap(__float_as_uint(o[10]), __float_as_uint(o[10]), false, false);
      l = __uint_as_float(sw[0]);
    }
    const float rl = 1.f / l;
    u32x4 ov;
#pragma unroll
    for (int e = 0; e < 4; ++e) ov[e] = nsent(cvtpk(o[2 * e] * rl, o[2 * e + 1] * rl));
    gstore16s(ws, WS_O1 + head * 65536 + (qt + kh) * 1024 + lane * 16, ov);
    if (!h) gstore4s(ws, WS_O2 + head * 8192 + ((qt + kh) * 32 + r32) * 4, nsent(cvtpk(o[8] * rl, o[9] * rl)));
    STAMP(8);
  }
  float wsum;
  {
#pragma unroll
    for (int i = 0; i < 5; ++i) PIN(wrow[i]);
    if (h) wrow[4] = 0u;
    float t = 0.f;
#pragma unroll
    for (int i = 0; i < 5; ++i) t = dot2(wrow[i], 0x3F803F80u, t);
    wsum = sum64(t);
  }
  f32x4 acc[5];
  {
    u32x4 of[9];
    const unsigned o1 = WS_O1 + (kq * 16 + (g >> 1)) * 65536 + (2 * panel + (mt >> 1)) * 1024 + (g & 1) * 512 + ((mt & 1) * 16 + l16) * 16;
    const int q = l16 >> 2;
    const lds_cptr bp = (lds_cptr)smem + (kq * 8 * 36 + 20 * (g >> 1) + 4 * (g & 1) + q) * 144 + (lane & 3) * 8;
    const lds_cptr bp2 = (lds_cptr)smem + (kq * 288 + 18 * (4 * g + (q >> 1)) + ((q >> 1) ? 0 : 16) + (q & 1)) * 144 + (lane & 3) * 8;
    bf16x8 wpf[25];
#pragma unroll
    for (int i = 0; i < 25; ++i) {
      wpf[i] = cat8(ldstr(bp + (i / 5) * (36 * 144) + (i % 5) * 32), ldstr(bp + (i / 5) * (36 * 144) + 8 * 144 + (i % 5) * 32));
      PIN(wpf[i]);
    }
#pragma unroll
    for (int i = 0; i < 9; ++i) of[i] = u32x4{SENT, SENT, SENT, SENT};
    {
      unsigned d[4] = {SENT, SENT, SENT, SENT};
      _Pragma("unroll 1") for (unsigned spin = 0; spin < SPIN_CAP; ++spin) {
        poll_o8(of, d, ws, o1, WS_O2 + kq * 0x20000 + g * 32768 + tok * 4);
        of[8] = u32x4{d[0], d[1], d[2], d[3]};
        unsigned m = 0;
#pragma unroll
        for (int i = 0; i < 9; ++i) m = max(m, mx4(of[i]));
        if (__all(m != SENT)) break;
        __builtin_amdgcn_s_sleep(1);
      }
    }
    STAMP(9);
#pragma unroll
    for (int i = 0; i < 5; ++i) acc[i] = f32x4{};
#pragma unroll
    for (int ks = 0; ks < 9; ++ks) {
#pragma unroll
      for (int i = 0; i < 5; ++i) acc[i] = MFMA16(wpf[(ks < 5 ? ks : ks - 4) * 5 + i], __builtin_bit_cast(bf16x8, of[ks]), acc[i]);
      if (ks >= 1 && ks < 5) {
        const lds_cptr b0 = ks == 4 ? bp2 : bp + (ks + 4) * (36 * 144);
        const int d2 = ks == 4 ? 36 * 144 : 8 * 144;
#pragma unroll
        for (int i = 0; i < 5; ++i) wpf[ks * 5 + i] = cat8(ldstr(b0 + i * 32), ldstr(b0 + d2 + i * 32));
      }
      __builtin_amdgcn_sched_barrier(0);
    }
    LAS f32x4* xw = (LAS f32x4*)(smem + P3_XOFF) + mt * 320 + lane;
    if (kq == 0) { xw[3 * 64] = acc[3]; xw[4 * 64] = acc[4]; } else { xw[0] = acc[0]; xw[64] = acc[1]; xw[128] = acc[2]; }
    LDS_BAR();
    if (kq == 0) { acc[0] += xw[0]; acc[1] += xw[64]; acc[2] += xw[128]; }
    else { acc[0] = acc[3] + xw[3 * 64]; acc[1] = acc[4] + xw[4 * 64]; acc[2] = f32x4{}; }
  }
#pragma unroll
  for (int i = 0; i < 3; ++i) { PIN(xres[i]); PIN(wrr[i]); }
  STAMP(10);
  const int rgp = panel * 4 + mt;
  float hv[3][4];
  unsigned key; float mb, rinv;
  {
    float s1 = 0.f, s2 = 0.f, s3 = 0.f;
#pragma unroll
    for (int i = 0; i < 3; ++i) {
      const bool valid = (i < nnt) && ((nt0 + i) * 16 + 4 * g < 72);
      const unsigned a01 = cvtpk(acc[i][0], acc[i][1]), a23 = cvtpk(acc[i][2], acc[i][3]);
      unsigned h01 = cvtpk(bflo(xres[i][0]) + bflo(a01), bfhi(xres[i][0]) + bfhi(a01));
      unsigned h23 = cvtpk(bflo(xres[i][1]) + bflo(a23), bfhi(xres[i][1]) + bfhi(a23));
      h01 = valid ? h01 : 0u; h23 = valid ? h23 : 0u;
      hv[i][0] = bflo(h01); hv[i][1] = bfhi(h01); hv[i][2] = bflo(h23); hv[i][3] = bfhi(h23);
      s1 += (hv[i][0] + hv[i][1]) + (hv[i][2] + hv[i][3]);
      s2 = fmaf(hv[i][0], hv[i][0], fmaf(hv[i][1], hv[i][1], fmaf(hv[i][2], hv[i][2], fmaf(hv[i][3], hv[i][3], s2))));
      s3 = fmaf(hv[i][0], bflo(wrr[i][0]), fmaf(hv[i][1], bfhi(wrr[i][0]), fmaf(hv[i][2], bflo(wrr[i][1]), fmaf(hv[i][3], bfhi(wrr[i][1]), s3))));
    }
    s1 = sum_x32(sum_x16(s1)); s2 = sum_x32(sum_x16(s2)); s3 = sum_x32(sum_x16(s3));
    if (g == 0) gstore16s(ws, SY_STATS + rgp * 4096 + (cg * 2 + kq) * 256 + l16 * 16,
                          u32x4{nsent(__float_as_uint(s1)), nsent(__float_as_uint(s2)), nsent(__float_as_uint(s3)), 0u});
    u32x4 sv[4];
#pragma unroll
    for (int i = 0; i < 4; ++i) sv[i] = u32x4{SENT, SENT, SENT, SENT};
    _Pragma("unroll 1") for (unsigned spin = 0; spin < SPIN_CAP; ++spin) {
      poll_4x16(sv, ws, SY_STATS + rgp * 4096 + lane * 16);
      if (__all(max(max(mx4(sv[0]), mx4(sv[1])), max(mx4(sv[2]), mx4(sv[3]))) != SENT)) break;
      __builtin_amdgcn_s_sleep(1);
    }
    STAMP(11);
#define SUMC(c) sum_x32(sum_x16((__uint_as_float(sv[0][c]) + __uint_as_float(sv[1][c])) + (__uint_as_float(sv[2][c]) + __uint_as_float(sv[3][c]))))
    s1 = SUMC(0); s2 = SUMC(1); s3 = SUMC(2);
    const float m = s1 * (1.f / 576.f);
    mb = rbf(m);
    rinv = 1.f / rbf(sqrtf(rbf(rbf(fmaxf(fmaf(s2, 1.f / 576.f, -m * m), 0.f)) + 1e-5f)));
    const unsigned u = __float_as_uint((s3 - mb * wsum) * rinv);
    const unsigned ord = (u & 0x80000000u) ? ~u : (u | 0x80000000u);
    key = (min(ord, 0xFFFFF000u) & ~0x7FFu) | (unsigned)(2047 - tok);
    key = max(key, dppu<0xB1>(key)); key = max(key, dppu<0x4E>(key)); key = max(key, dppu<0x141>(key)); key = max(key, dppu<0x140>(key));
  }
  if (cg == 0 && kq == 0 && lane < 16) gstore4s(ws, SY_BEST + lane * 4096 + rgp * 4, key);
  const bool isbest = tok == 2047 - (int)(key & 0x7FFu);
  u32x2 ov[3];
#pragma unroll
  for (int i = 0; i < 3; ++i) {
    const bool valid = (i < nnt) && ((nt0 + i) * 16 + 4 * g < 72);
    const unsigned d01 = cvtpk(hv[i][0] - mb, hv[i][1] - mb), d23 = cvtpk(hv[i][2] - mb, hv[i][3] - mb);
    const unsigned n01 = cvtpk(bflo(d01) * rinv, bfhi(d01) * rinv), n23 = cvtpk(bflo(d23) * rinv, bfhi(d23) * rinv);
    ov[i] = u32x2{cvtpk(hv[i][0] + bflo(n01), hv[i][1] + bfhi(n01)), cvtpk(hv[i][2] + bflo(n23), hv[i][3] + bfhi(n23))};
    const int col = cg * 72 + (nt0 + i) * 16 + 4 * g;
    if (valid && isbest) {
      gstore8s(ws, SY_CHN + rgp * 1152 + col * 2, u32x2{nsent(n01), nsent(n23)});
      gstore8s(ws, SY_CH + rgp * 1152 + col * 2, u32x2{nsent(cvtpk(hv[i][0], hv[i][1])), nsent(cvtpk(hv[i][2], hv[i][3]))});
    }
  }
  if (w != 0 && !isbest) {
#pragma unroll
    for (int i = 0; i < 3; ++i) {
      const bool valid = (i < nnt) && ((nt0 + i) * 16 + 4 * g < 72);
      if (valid) *(u32x2*)(out + (size_t)tok * 1152 + (cg * 72 + (nt0 + i) * 16 + 4 * g) * 2) = ov[i];
    }
  }
  float gf[9][8], df[8][8];
#pragma unroll
  for (int i = 0; i < 9; ++i) {
    PIN(gw[i]); PIN(uw[i]);
    gf[i][0] = bflo(gw[i][0]); gf[i][1] = bfhi(gw[i][0]); gf[i][2] = bflo(gw[i][1]); gf[i][3] = bfhi(gw[i][1]);
    gf[i][4] = bflo(uw[i][0]); gf[i][5] = bfhi(uw[i][0]); gf[i][6] = bflo(uw[i][1]); gf[i][7] = bfhi(uw[i][1]);
  }
#pragma unroll
  for (int i = 0; i < 8; ++i) {
    PIN(dw[i]);
#pragma unroll
    for (int e = 0; e < 4; ++e) { df[i][2 * e] = bflo(dw[i][e]); df[i][2 * e + 1] = bfhi(dw[i][e]); }
  }
#pragma unroll
  for (int i = 0; i < 72; ++i) PIN(gf[i >> 3][i & 7]);
#pragma unroll
  for (int i = 0; i < 64; ++i) PIN(df[i >> 3][i & 7]);
  LAS unsigned* xs = (LAS unsigned*)(smem + P3_SOFF + 8192);
  if (w == 0) {
    u32x2 a = u32x2{SENT, SENT};
    _Pragma("unroll 1") for (unsigned spin = 0; spin < SPIN_CAP; ++spin) {
      poll_8(a, ws, SY_BEST + (b & 15) * 4096 + lane * 8);
      if (__all(max(a[0], a[1]) != SENT)) break;
      __builtin_amdgcn_s_sleep(1);
    }
    const int s0 = __builtin_amdgcn_readfirstlane(2047 - (int)(umax64(max(a[0], a[1])) & 0x7FFu));
    STAMP(12);
    if (b < 180) {
      unsigned xv[5] = {SENT, SENT, SENT, SENT, SENT};
      _Pragma("unroll 1") for (unsigned spin = 0; spin < SPIN_CAP; ++spin) {
        poll_5x4(xv, ws, SY_CHN + (s0 >> 4) * 1152 + lane * 4, SY_CHN + (s0 >> 4) * 1152 + 1024 + r32 * 4);
        unsigned m = 0;
#pragma unroll
        for (int i = 0; i < 5; ++i) m = max(m, xv[i]);
        if (__all(m != SENT)) break;
      }
#pragma unroll
      for (int i = 0; i < 5; ++i) { xs[i * 64 + lane] = xv[i]; *(LAS u32x2*)(xs + 384 + i * 128 + lane * 2) = u32x2{xv[i] << 16, xv[i] & 0xffff0000u}; }
    }
    xs[320] = s0;
  }
  LDS_BAR();
  const int sel = __builtin_amdgcn_readfirstlane(xs[320]);
  const int wgrp = sel >> 4;
  if (ffn) {
    STAMP(13);
    float ga[8] = {0.f, 0.f, 0.f, 0.f, 0.f, 0.f, 0.f, 0.f};
#pragma unroll
    for (int i = 0; i < 9; ++i) {
      const float xf = __uint_as_float(xs[384 + (tid >> 3) + 64 * i]);
#pragma unroll
      for (int e = 0; e < 8; ++e) ga[e] = fmaf(xf, gf[i][e], ga[e]);
    }
    LAS float* gp = (LAS float*)(smem + P3_SOFF);
    {
      const float r0 = red32(ga[0], ga[4]), r1 = red32(ga[1], ga[5]), r2 = red32(ga[2], ga[6]), r3 = red32(ga[3], ga[7]);
      *(LAS u32x2*)(gp + (w * 2 + ((lane >> 3) & 1)) * 64 + h * 32 + (lane & 7) * 4 + ((lane >> 4) & 1) * 2) =
          u32x2{__float_as_uint(red16(r0, r2)), __float_as_uint(red16(r1, r3))};
    }
    LDS_BAR();
    STAMP(18);
    float av;
    {
      float s = 0.f;
#pragma unroll
      for (int i = 0; i < 16; ++i) s += gp[i * 64 + lane];
      s = rbf(s);
      auto sw = __builtin_amdgcn_permlane32_swap(__float_as_uint(s), __float_as_uint(s), false, false);
      const float gs = __uint_as_float(sw[0]), us = __uint_as_float(sw[1]);
      av = rbf(rbf(gs / (1.f + __expf(-gs))) * us);
    }
    float y[8] = {0.f, 0.f, 0.f, 0.f, 0.f, 0.f, 0.f, 0.f};
#pragma unroll
    for (int i = 0; i < 8; ++i) {
      const float a = __shfl(av, g * 8 + i);
#pragma unroll
      for (int e = 0; e < 8; ++e) y[e] = fmaf(a, df[i][e], y[e]);
    }
    const float r0 = red32(y[0], y[4]), r1 = red32(y[1], y[5]), r2 = red32(y[2], y[6]), r3 = red32(y[3], y[7]);
    const float y0 = red16(r0, r2), y1 = red16(r1, r3);
    STAMP(19);
    if (l16 < 9) gstore8s(ws, SY_PART + fs * 2304 + ((l16 < 8 ? w * 64 + l16 * 8 : 512 + w * 8) + g * 2) * 4, u32x2{nsent(__float_as_uint(y0)), nsent(__float_as_uint(y1))});
  }
  if (tok != sel && (w == 0 || isbest)) {
#pragma unroll
    for (int i = 0; i < 3; ++i) {
      const bool valid = (i < nnt) && ((nt0 + i) * 16 + 4 * g < 72);
      if (valid) *(u32x2*)(out + (size_t)tok * 1152 + (cg * 72 + (nt0 + i) * 16 + 4 * g) * 2) = ov[i];
    }
  }
  if (!ffn && b < 180 && w == 1) {
    const int c0 = (b - 144) * 16 + (lane & 3) * 4;
    float r = 0.f;
#pragma unroll
    for (int i = 0; i < 5; ++i) { const unsigned xv = xs[i * 64 + lane]; r = fmaf(bflo(xv), bflo(wrow[i]), fmaf(bfhi(xv), bfhi(wrow[i]), r)); }
    const unsigned n01 = xs[c0 >> 1], n23 = xs[(c0 >> 1) + 1];
    r = rbf(sum64(r));
    const float sig = rbf(1.f / (1.f + __expf(-r)));
    STAMP(13);
    u32x4 pv[9];
#pragma unroll
    for (int i = 0; i < 9; ++i) pv[i] = u32x4{SENT, SENT, SENT, SENT};
    u32x2 hp = u32x2{SENT, SENT};
    _Pragma("unroll 1") for (unsigned spin = 0; spin < SPIN_CAP; ++spin) {
      poll_part(pv, hp, ws, SY_PART + (lane >> 2) * 2304 + c0 * 4, SY_CH + wgrp * 1152 + c0 * 2);
      unsigned m = max(hp[0], hp[1]);
#pragma unroll
      for (int i = 0; i < 9; ++i) m = max(m, mx4(pv[i]));
      if (__all(m != SENT)) break;
    }
    float y[4];
#pragma unroll
    for (int e = 0; e < 4; ++e) {
      float t = 0.f;
#pragma unroll
      for (int i = 0; i < 9; ++i) t += __uint_as_float(pv[i][e]);
      t += dppf<0x124>(t); t += dppf<0x128>(t);
      y[e] = rbf(rbf(sum_x32(sum_x16(t))) * sig);
    }
    if (lane < 4) *(u32x2*)(out + (size_t)sel * 1152 + c0 * 2) =
        u32x2{cvtpk(bflo(hp[0]) + rbf(bflo(n01) + y[0]), bfhi(hp[0]) + rbf(bfhi(n01) + y[1])),
              cvtpk(bflo(hp[1]) + rbf(bflo(n23) + y[2]), bfhi(hp[1]) + rbf(bfhi(n23) + y[3]))};
    if (b == 144 && lane == 0) st_u64(ws + WS_EPOCH, tag);
  }
  STAMP_FLUSH(0);
}

extern "C" void kernel_launch(void* const* d_in, const int* in_sizes, int n_in, void* d_out, int out_size, void* d_ws,
                              size_t ws_size, hipStream_t stream) {
  (void)hipFuncSetAttribute((const void*)k_block, hipFuncAttributeMaxDynamicSharedMemorySize, LDS_BYTES);
  k_block<<<256, 512, LDS_BYTES, stream>>>((const char*)d_in[0], (const char*)d_in[1], (const char*)d_in[2], (const char*)d_in[4],
                                           (const char*)d_in[5], (const char*)d_in[6], (const char*)d_in[7], (char*)d_out, (char*)d_ws);
}
```
